```python
import torch

def _softmax(x, axis=-1):
    return torch.softmax(x, dim=axis)
def _gelu(x):
    return torch.nn.functional.gelu(x, approximate="tanh")
def _silu(x):
    return torch.nn.functional.silu(x)
def _sigmoid(x):
    return torch.sigmoid(x)
def _layernorm(x, axis=-1, eps=1e-5):
    m = x.mean(dim=axis, keepdim=True)
    v = x.var(dim=axis, keepdim=True, unbiased=False)
    return (x - m) / torch.sqrt(v + eps)
def _scatter_add(tgt, idx, src, axis=0):
    return tgt.clone().index_add_(axis, idx, src)
def _segment_sum(src, seg, n):
    out = torch.zeros((n,) + src.shape[1:], dtype=src.dtype)
    return out.index_add_(0, seg, src)
def _pad(x, pw):
    flat = tuple(v for pair in reversed(pw) for v in pair)
    return torch.nn.functional.pad(x, flat)

kernel_name = 'concat_heads__ssm_conv_s4d__linear_kernel_hedgehog__prompt'

def setup_inputs():

    _w = (lambda a: (lambda t: t.to(torch.bfloat16) if t.is_floating_point() else t)(torch.as_tensor(a)))
    def _A_setup():
        Bsz, L, D, N = 1, 2048, 448, 256
        return {
            "u": _w((lambda *s: torch.randn(*s, dtype=torch.bfloat16) * 0.05)(Bsz, L, D)),
            "log_A_real": _w((lambda *s: torch.randn(*s, dtype=torch.bfloat16) * 6.4)(D, N) * 0.5),
            "A_imag": _w((lambda *s: torch.randn(*s, dtype=torch.bfloat16) * 0.2)(D, N) * 3.14),
            "C_real": _w((lambda *s: torch.randn(*s, dtype=torch.bfloat16) * 0.8)(D, N) / N ** 0.5),
            "C_imag": _w((lambda *s: torch.randn(*s, dtype=torch.bfloat16) * 0.8)(D, N) / N ** 0.5),
            "log_dt": _w((lambda *s: torch.randn(*s, dtype=torch.bfloat16) * 0.2)(D) * 0.5 - 4.0),
            "D_skip": _w((lambda *s: torch.randn(*s, dtype=torch.bfloat16) * 0.2)(D)),
        }
    _A_in = _A_setup()

    def _B_setup():
        B, S, D, H = 1, 2048, 448, 16
        Dh = D // H
        return {
            "x": _w((lambda *s: torch.randn(*s, dtype=torch.bfloat16) * 0.4)(B, S, D)),
            "wqkv": _w((lambda *s: torch.randn(*s, dtype=torch.bfloat16) * 0.4)(D, 3 * D)),
            "w_phi": _w((lambda *s: torch.randn(*s, dtype=torch.bfloat16) * 0.4)(H, Dh, Dh) * (Dh ** -0.5)),
            "wo": _w((lambda *s: torch.randn(*s, dtype=torch.bfloat16) * 0.05)(D, D)),
        }
    _B_in = _B_setup()

    return {
        "A_u": _A_in["u"],
        "A_log_A_real": _A_in["log_A_real"],
        "A_A_imag": _A_in["A_imag"],
        "A_C_real": _A_in["C_real"],
        "A_C_imag": _A_in["C_imag"],
        "A_log_dt": _A_in["log_dt"],
        "A_D_skip": _A_in["D_skip"],
        "B_x": _B_in["x"],
        "B_wqkv": _B_in["wqkv"],
        "B_w_phi": _B_in["w_phi"],
        "B_wo": _B_in["wo"],
    }

def reference(A_u, A_log_A_real, A_A_imag, A_C_real, A_C_imag, A_log_dt, A_D_skip, B_x, B_wqkv, B_w_phi, B_wo):
    def _A_ref(u, log_A_real, A_imag, C_real, C_imag, log_dt, D_skip):
        L = u.shape[1]
        dt = torch.exp(log_dt)
        A = -torch.exp(log_A_real) + 1j * A_imag
        C = (C_real + 1j * C_imag) * dt[:, None]
        pos = (lambda x, dt: x.to(dt))(torch.arange(L), log_dt.dtype)
        P = torch.exp((A * dt[:, None])[:, :, None] * pos[None, None, :])
        K = 2.0 * torch.einsum("dn,dnl->dl", C, P).real
        L2 = 2 * L
        u_t = (lambda x, *d: x.permute(*d))(u, 0, 2, 1)
        Kf = (lambda x, *a, **k: torch.fft.rfft(x.float(), *a, **k))(K, L2)
        Uf = (lambda x, *a, **k: torch.fft.rfft(x.float(), *a, **k))(u_t, L2)
        y = (lambda x, *a, **k: torch.fft.irfft(x, *a, **k).to(torch.bfloat16))(Uf * Kf[None, :, :], L2)[:, :, :L]
        y = (lambda x, *d: x.permute(*d))(y, 0, 2, 1)
        return y + u * D_skip[None, None, :]

    def _B_ref(x, wqkv, w_phi, wo):
        B, S, D = x.shape
        H = 16; Dh = D // H
        qkv = (lambda x, *d: x.permute(*d))((x @ wqkv).reshape(B, S, 3, H, Dh), 2, 0, 3, 1, 4)
        q, k, v = qkv[0], qkv[1], qkv[2]
        pq = torch.einsum("bhsd,hdf->bhsf", q, w_phi)
        pk = torch.einsum("bhsd,hdf->bhsf", k, w_phi)
        phi_q = torch.concatenate([_softmax(pq, axis=-1), _softmax(-pq, axis=-1)], axis=-1)
        phi_k = torch.concatenate([_softmax(pk, axis=-1), _softmax(-pk, axis=-1)], axis=-1)
        kv = torch.einsum("bhsf,bhsd->bhfd", phi_k, v)
        z  = torch.einsum("bhsf->bhf", phi_k)
        num = torch.einsum("bhqf,bhfd->bhqd", phi_q, kv)
        den = torch.einsum("bhqf,bhf->bhq", phi_q, z)[..., None] + 1e-6
        o = num / den
        return (lambda x, *d: x.permute(*d))(o, 0, 2, 1, 3).reshape(B, S, D) @ wo

    a = _A_ref(A_u, A_log_A_real, A_A_imag, A_C_real, A_C_imag, A_log_dt, A_D_skip)
    b = _B_ref(B_x, B_wqkv, B_w_phi, B_wo)
    return torch.concatenate([a, b], axis=-1)
```

```cpp
#include <hip/hip_runtime.h>
#include <hip/hip_bf16.h>
#include <stdint.h>

typedef unsigned short u16;
typedef __bf16 bf16x8 __attribute__((ext_vector_type(8)));
typedef float f32x4 __attribute__((ext_vector_type(4)));
typedef short s16x4 __attribute__((ext_vector_type(4)));
typedef unsigned u32x4 __attribute__((ext_vector_type(4)));
typedef unsigned u32x2 __attribute__((ext_vector_type(2)));
#define LDS_AS __attribute__((address_space(3)))
#define DI __device__ __forceinline__

constexpr int L = 2048, D = 448, NST = 256, H = 16, DH = 28;
constexpr int OUTW = 2 * D;

constexpr int KREV_LEN = 2320;
constexpr int KOFF = 2064;
constexpr size_t WS_UT = 0;
constexpr size_t WS_YT = WS_UT + (size_t)D * L * 2;
constexpr size_t WS_KREV = WS_YT + (size_t)D * L * 2;
constexpr size_t WS_KVP = WS_KREV + (size_t)D * 2 * KREV_LEN * 2;
constexpr size_t WS_O = WS_KVP + (size_t)H * 16 * 2048 * 4;
constexpr size_t WS_END = WS_O + (size_t)L * D * 2;

DI float bf2f(u16 v) { return __uint_as_float(((unsigned)v) << 16); }
DI u16 f2bf(float f) { __bf16 b = (__bf16)f; return __builtin_bit_cast(u16, b); }
DI float rbf(float f) { return bf2f(f2bf(f)); }
DI unsigned pack2(float a, float b) { return (unsigned)f2bf(a) | ((unsigned)f2bf(b) << 16); }
DI f32x4 mfma16(u32x4 a, u32x4 b, f32x4 c) {
  return __builtin_amdgcn_mfma_f32_16x16x32_bf16(__builtin_bit_cast(bf16x8, a), __builtin_bit_cast(bf16x8, b), c, 0, 0, 0);
}
template <int CTRL> DI float dppf(float x) {
  return __builtin_bit_cast(float, __builtin_amdgcn_update_dpp(0, __builtin_bit_cast(int, x), CTRL, 0xf, 0xf, false));
}
DI float row16_max(float x) {
  x = fmaxf(x, dppf<0x128>(x)); x = fmaxf(x, dppf<0x124>(x)); x = fmaxf(x, dppf<0x122>(x)); x = fmaxf(x, dppf<0x121>(x)); return x;
}
DI float row16_min(float x) {
  x = fminf(x, dppf<0x128>(x)); x = fminf(x, dppf<0x124>(x)); x = fminf(x, dppf<0x122>(x)); x = fminf(x, dppf<0x121>(x)); return x;
}
DI float row16_sum(float x) {
  x += dppf<0x128>(x); x += dppf<0x124>(x); x += dppf<0x122>(x); x += dppf<0x121>(x); return x;
}
DI u32x2 tr_read(const char* lds, unsigned off) {
  s16x4 v = __builtin_amdgcn_ds_read_tr16_b64_v4i16((LDS_AS s16x4*)(lds + off));
  return __builtin_bit_cast(u32x2, v);
}

DI unsigned wimg_g(unsigned row) { return ((row >> 1) & 1u) | (((row >> 3) & 1u) << 1); }

template <int NPARTS>
DI void stage_w28(const u16* __restrict__ w, int ldw, int col0, int col1, char* lds) {
  const int tid = threadIdx.x;
  constexpr int SL = NPARTS * 8;
  constexpr int TOT = 448 * SL;
  constexpr int IT = TOT / 256;
  u32x2 v[IT];
#pragma unroll
  for (int it = 0; it < IT; ++it) {
    const int idx = tid + 256 * it;
    const int row = idx / SL, s = idx % SL;
    const int part = s >> 3, i = s & 7;
    u32x2 t = {0u, 0u};
    if (i < 7) t = *(const u32x2*)(w + (size_t)row * ldw + (part ? col1 : col0) + 4 * i);
    v[it] = t;
  }
#pragma unroll
  for (int it = 0; it < IT; ++it) {
    const int idx = tid + 256 * it;
    const int row = idx / SL, s = idx % SL;
    const unsigned chunk = (unsigned)(s >> 2);
    *(u32x2*)(lds + row * 128 + ((chunk ^ wimg_g(row)) << 5) + ((s & 3) << 3)) = v[it];
  }
}

struct WFragAddr { unsigned base; unsigned g; };
DI WFragAddr wfrag_addr(int lane) {
  const int kq = lane >> 4, q4 = (lane & 15) >> 2, p = lane & 3;
  WFragAddr a;
  a.base = (unsigned)((8 * kq + q4) * 128 + p * 8);
  a.g = (unsigned)((q4 >> 1) | ((kq & 1) << 1));
  return a;
}
DI u32x4 wfrag(const char* lds, const WFragAddr& a, int ct, int t) {
  const unsigned o = a.base + (((unsigned)ct ^ a.g) << 5) + (unsigned)t * 4096u;
  u32x2 lo = tr_read(lds, o), hi = tr_read(lds, o + 512u);
  u32x4 r = {lo[0], lo[1], hi[0], hi[1]};
  return r;
}

DI u32x4 wphi_frag(const u16* __restrict__ wphi_h, int lane, int ft) {
  const int f = (lane & 15) + 16 * ft, kq = lane >> 4;
  unsigned e[8];
#pragma unroll
  for (int j = 0; j < 8; ++j) {
    const int d = (j < 4) ? (4 * kq + j) : (16 + 4 * kq + j - 4);
    e[j] = (d < DH && f < DH) ? (unsigned)wphi_h[d * DH + f] : 0u;
  }
  u32x4 r = {e[0] | (e[1] << 16), e[2] | (e[3] << 16), e[4] | (e[5] << 16), e[6] | (e[7] << 16)};
  return r;
}
DI u32x4 pack8(const f32x4& a, const f32x4& b) {
  u32x4 r = {pack2(a[0], a[1]), pack2(a[2], a[3]), pack2(b[0], b[1]), pack2(b[2], b[3])};
  return r;
}

DI void role_b1(int bid, const u16* __restrict__ x, const u16* __restrict__ wqkv, const u16* __restrict__ wphi,
                char* ws, char* lds) {
  const int h = bid & 15, chunk = bid >> 4;
  const int tid = threadIdx.x, lane = tid & 63, wv = tid >> 6;
  const int r = lane & 15, kq = lane >> 4;
  const int row0 = chunk * 128 + wv * 32;
  u32x4 xf[14][2];
#pragma unroll
  for (int t = 0; t < 14; ++t)
#pragma unroll
    for (int rt = 0; rt < 2; ++rt)
      xf[t][rt] = *(const u32x4*)(x + (size_t)(row0 + 16 * rt + r) * D + 32 * t + 8 * kq);
  stage_w28<2>(wqkv, 3 * D, D + DH * h, 2 * D + DH * h, lds);
  const u16* wphi_h = wphi + h * DH * DH;
  const u32x4 wp0 = wphi_frag(wphi_h, lane, 0), wp1 = wphi_frag(wphi_h, lane, 1);
  __syncthreads();
  const WFragAddr wa = wfrag_addr(lane);
  f32x4 kT[2][2], vv[2][2];
#pragma unroll
  for (int i = 0; i < 2; ++i)
#pragma unroll
    for (int j = 0; j < 2; ++j) { kT[i][j] = f32x4{0, 0, 0, 0}; vv[i][j] = f32x4{0, 0, 0, 0}; }
#pragma unroll
  for (int t = 0; t < 14; ++t) {
#pragma unroll
    for (int ct = 0; ct < 2; ++ct) {
      const u32x4 wk = wfrag(lds, wa, ct, t);
      const u32x4 wvv = wfrag(lds, wa, 2 + ct, t);
#pragma unroll
      for (int rt = 0; rt < 2; ++rt) {
        kT[ct][rt] = mfma16(wk, xf[t][rt], kT[ct][rt]);
        vv[rt][ct] = mfma16(xf[t][rt], wvv, vv[rt][ct]);
      }
    }
  }
  float php[2][2][4], phn[2][2][4];
  const bool valid1 = r < (DH - 16);
#pragma unroll
  for (int rt = 0; rt < 2; ++rt) {
    const u32x4 ka = pack8(kT[0][rt], kT[1][rt]);
    f32x4 p0 = mfma16(ka, wp0, f32x4{0, 0, 0, 0});
    f32x4 p1 = mfma16(ka, wp1, f32x4{0, 0, 0, 0});
#pragma unroll
    for (int g = 0; g < 4; ++g) {
      const float a = rbf(p0[g]), b = rbf(p1[g]);
      const float mx = row16_max(valid1 ? fmaxf(a, b) : a);
      const float mn = row16_min(valid1 ? fminf(a, b) : a);
      const float ea = __expf(a - mx), eb = valid1 ? __expf(b - mx) : 0.f;
      const float na = __expf(mn - a), nb = valid1 ? __expf(mn - b) : 0.f;
      const float sp = row16_sum(ea + eb), sn = row16_sum(na + nb);
      const float ip = 1.f / sp, in = 1.f / sn;
      php[rt][0][g] = ea * ip; php[rt][1][g] = eb * ip;
      phn[rt][0][g] = na * in; phn[rt][1][g] = nb * in;
    }
  }
  u32x4 vb[2];
#pragma unroll
  for (int dt = 0; dt < 2; ++dt) vb[dt] = pack8(vv[0][dt], vv[1][dt]);
  if (r == DH - 16) vb[1] = u32x4{0x3f803f80u, 0x3f803f80u, 0x3f803f80u, 0x3f803f80u};
  f32x4 kv[4][2];
#pragma unroll
  for (int ft = 0; ft < 2; ++ft) {
    const u32x4 ap = {pack2(php[0][ft][0], php[0][ft][1]), pack2(php[0][ft][2], php[0][ft][3]),
                      pack2(php[1][ft][0], php[1][ft][1]), pack2(php[1][ft][2], php[1][ft][3])};
    const u32x4 an = {pack2(phn[0][ft][0], phn[0][ft][1]), pack2(phn[0][ft][2], phn[0][ft][3]),
                      pack2(phn[1][ft][0], phn[1][ft][1]), pack2(phn[1][ft][2], phn[1][ft][3])};
#pragma unroll
    for (int dt = 0; dt < 2; ++dt) {
      kv[ft][dt] = mfma16(ap, vb[dt], f32x4{0, 0, 0, 0});
      kv[2 + ft][dt] = mfma16(an, vb[dt], f32x4{0, 0, 0, 0});
    }
  }
  __syncthreads();
  float* red = (float*)lds;
#pragma unroll
  for (int f4 = 0; f4 < 4; ++f4)
#pragma unroll
    for (int dt = 0; dt < 2; ++dt)
#pragma unroll
      for (int g = 0; g < 4; ++g)
        red[wv * 2048 + (16 * f4 + 4 * kq + g) * 32 + 16 * dt + r] = kv[f4][dt][g];
  __syncthreads();
  float* dst = (float*)(ws + WS_KVP) + (size_t)(h * 16 + chunk) * 2048;
#pragma unroll
  for (int i = 0; i < 2; ++i) {
    const int e = (tid + 256 * i) * 4;
    f32x4 s = *(const f32x4*)(red + e);
    s += *(const f32x4*)(red + 2048 + e);
    s += *(const f32x4*)(red + 4096 + e);
    s += *(const f32x4*)(red + 6144 + e);
    *(f32x4*)(dst + e) = s;
  }
}

struct cplx { float re, im; };
DI cplx cmul(cplx a, cplx b) { return cplx{a.re * b.re - a.im * b.im, a.re * b.im + a.im * b.re}; }

constexpr int KG_STRIDE = 544;
constexpr int KG_WAVE = 32 * KG_STRIDE;

DI u32x2 split_entry(float re, float im) {
  const unsigned h = pack2(re, im);
  const float rr = re - __uint_as_float(h << 16), ri = im - __uint_as_float(h & 0xffff0000u);
  u32x2 o = {h, pack2(rr, ri)};
  return o;
}

DI int kpos_index(int l) {
  int pos = l;
  if (l >= 256) {
    const int sh = (l >= 1024) ? 3 : (l >= 512 ? 2 : 1);
    const int half = 1 << (sh - 1);
    const int lsb = (l >> sh) & 1;
    pos = ((l + half - 1 + lsb) >> sh) << sh;
  }
  int row;
  const int j = (pos >> 3) & 15;
  if (pos < 256) row = ((pos >> 7) << 3) + (pos & 7);
  else if (pos < 512) row = 16 + (((pos >> 7) & 1) << 2) + ((pos >> 1) & 3);
  else if (pos < 1024) row = 24 + (((pos >> 7) & 3) << 1) + ((pos >> 2) & 1);
  else if (pos < 2048) row = 32 + ((pos >> 7) & 7);
  else row = 40;
  return row * 16 + j;
}

DI void role_kgen(int d, const u16* __restrict__ logAre, const u16* __restrict__ Aim, const u16* __restrict__ Cre,
                  const u16* __restrict__ Cim, const u16* __restrict__ logdt, char* ws, char* lds) {
  const int tid = threadIdx.x, lane = tid & 63, wv = tid >> 6;
  const int n = tid;
  const float dt = rbf(expf(bf2f(logdt[d])));
  const float are = -rbf(expf(bf2f(logAre[d * NST + n])));
  const float ar = are * dt;
  const float ai = bf2f(Aim[d * NST + n]) * dt;
  const cplx C2 = {2.f * (bf2f(Cre[d * NST + n]) * dt), 2.f * (bf2f(Cim[d * NST + n]) * dt)};
  const float L2E = 1.44269502162933349609f, L2E_LO = 1.92596299112661746e-8f;
  const float I2P = 0.15915494263172149658f, I2P_LO = 6.4206383e-9f;
  const float LN2 = 0.69314718055994530942f;
  const float xh = ar * L2E;
  const float xl = fmaf(ar, L2E, -xh) + ar * L2E_LO;
  const float th = ai * I2P;
  const float tl = fmaf(ai, I2P, -th) + ai * I2P_LO;
  auto cexp = [&](float m) -> cplx {
    const float mag = __builtin_amdgcn_exp2f(xh * m) * fmaf(xl * m, LN2, 1.f);
    const float T = th * m;
    const float rr = (T - __builtin_rintf(T)) + tl * m;
    return cplx{mag * __builtin_amdgcn_cosf(rr), mag * __builtin_amdgcn_sinf(rr)};
  };
  char* wl = lds + wv * KG_WAVE;
  char* fine = wl + n % 64 * 8;
  char* coarse = wl + 16 * KG_STRIDE + (n % 64) * 8;
  {
    cplx F[16];
    F[0] = cplx{1.f, 0.f};
    F[1] = cexp(8.f); F[2] = cexp(16.f); F[4] = cexp(32.f); F[8] = cexp(64.f);
    F[3] = cmul(F[2], F[1]); F[5] = cmul(F[4], F[1]); F[6] = cmul(F[4], F[2]); F[7] = cmul(F[4], F[3]);
#pragma unroll
    for (int j = 1; j < 8; ++j) F[8 + j] = cmul(F[8], F[j]);
#pragma unroll
    for (int j = 0; j < 16; ++j) *(u32x2*)(fine + j * KG_STRIDE) = split_entry(F[j].re, F[j].im);
  }
  cplx E[8];
  E[0] = cplx{1.f, 0.f};
  E[1] = cexp(1.f); E[2] = cexp(2.f); E[4] = cexp(4.f);
  E[3] = cmul(E[2], E[1]); E[5] = cmul(E[4], E[1]); E[6] = cmul(E[4], E[2]); E[7] = cmul(E[4], E[3]);
  cplx Hh[17];
  {
    const cplx X1 = cexp(128.f), X2 = cexp(256.f), X4 = cexp(512.f), X8 = cexp(1024.f), X16 = cexp(2048.f);
    Hh[0] = C2; Hh[1] = cmul(C2, X1); Hh[2] = cmul(C2, X2); Hh[3] = cmul(Hh[2], X1);
    Hh[4] = cmul(C2, X4); Hh[5] = cmul(Hh[4], X1); Hh[6] = cmul(Hh[4], X2); Hh[7] = cmul(Hh[6], X1);
    Hh[8] = cmul(C2, X8);
    Hh[9] = cmul(Hh[8], X1); Hh[10] = cmul(Hh[8], X2); Hh[11] = cmul(Hh[10], X1);
    Hh[12] = cmul(Hh[8], X4); Hh[13] = cmul(Hh[12], X1); Hh[14] = cmul(Hh[12], X2); Hh[15] = cmul(Hh[14], X1);
    Hh[16] = cmul(C2, X16);
  }
  const int fi = lane & 15, kq = lane >> 4;
  const char* fr_base = wl + fi * KG_STRIDE + kq * 16;
  f32x4 acc[3];
#pragma unroll
  for (int tile = 0; tile < 3; ++tile) {
#pragma unroll
    for (int rr = 0; rr < 16; ++rr) {
      cplx g;
      const int row = tile * 16 + rr;
      if (row < 16) { const int i = row >> 3, e = row & 7; g = e ? cmul(Hh[i], E[e]) : Hh[i]; }
      else if (row < 24) { const int i = (row - 16) >> 2, e = (row - 16) & 3; g = e ? cmul(Hh[2 + i], E[2 * e]) : Hh[2 + i]; }
      else if (row < 32) { const int i = (row - 24) >> 1, e = (row - 24) & 1; g = e ? cmul(Hh[4 + i], E[4]) : Hh[4 + i]; }
      else if (row < 41) g = Hh[8 + row - 32];
      else g = cplx{0.f, 0.f};
      *(u32x2*)(coarse + rr * KG_STRIDE) = split_entry(g.re, -g.im);
    }
    f32x4 a = {0, 0, 0, 0};
#pragma unroll
    for (int tp = 0; tp < 8; ++tp) {
      const u32x4 fb = *(const u32x4*)(fr_base + tp * 64);
      const u32x4 ga = *(const u32x4*)(fr_base + 16 * KG_STRIDE + tp * 64);
      a = mfma16(u32x4{ga[0], ga[0], ga[1], ga[1]}, u32x4{fb[0], fb[1], fb[0], fb[1]}, a);
      a = mfma16(u32x4{ga[2], ga[2], ga[3], ga[3]}, u32x4{fb[2], fb[3], fb[2], fb[3]}, a);
    }
    acc[tile] = a;
  }
  __syncthreads();
  float* red = (float*)lds;
#pragma unroll
  for (int tile = 0; tile < 3; ++tile)
#pragma unroll
    for (int g = 0; g < 4; ++g) red[wv * 768 + (16 * tile + 4 * kq + g) * 16 + fi] = acc[tile][g];
  __syncthreads();
  if (tid < 192) {
    f32x4 s = *(const f32x4*)(red + tid * 4);
    s += *(const f32x4*)(red + 768 + tid * 4);
    s += *(const f32x4*)(red + 1536 + tid * 4);
    s += *(const f32x4*)(red + 2304 + tid * 4);
    *(f32x4*)(red + 3072 + tid * 4) = s;
  }
  __syncthreads();
  const float* Kc = red + 3072;
  u16* khi = (u16*)(ws + WS_KREV) + (size_t)d * 2 * KREV_LEN;
  u16* klo = khi + KREV_LEN;
  for (int g8 = tid; g8 < KREV_LEN / 8; g8 += 256) {
    unsigned hh[4], ll[4];
#pragma unroll
    for (int p = 0; p < 4; ++p) {
      float v[2];
#pragma unroll
      for (int e = 0; e < 2; ++e) {
        const int l = KOFF - (g8 * 8 + 2 * p + e);
        v[e] = (l >= 0 && l < L) ? Kc[kpos_index(l)] : 0.f;
      }
      const u32x2 s = split_entry(v[0], v[1]);
      hh[p] = s[0]; ll[p] = s[1];
    }
    *(u32x4*)(khi + g8 * 8) = u32x4{hh[0], hh[1], hh[2], hh[3]};
    *(u32x4*)(klo + g8 * 8) = u32x4{ll[0], ll[1], ll[2], ll[3]};
  }
}

DI void role_utrans(int tile, const u16* __restrict__ u, char* ws, char* lds) {
  const int lt = tile / 7, dtile = tile % 7;
  const int tid = threadIdx.x;
  u16* t = (u16*)lds;
#pragma unroll
  for (int i = 0; i < 2; ++i) {
    const int row = (tid >> 3) + 32 * i, ch = tid & 7;
    const u32x4 v = *(const u32x4*)(u + (size_t)(lt * 64 + row) * D + dtile * 64 + ch * 8);
#pragma unroll
    for (int j = 0; j < 4; ++j) {
      t[(ch * 8 + 2 * j) * 72 + row] = (u16)(v[j] & 0xffffu);
      t[(ch * 8 + 2 * j + 1) * 72 + row] = (u16)(v[j] >> 16);
    }
  }
  __syncthreads();
  u16* uT = (u16*)(ws + WS_UT);
#pragma unroll
  for (int i = 0; i < 2; ++i) {
    const int drow = (tid >> 3) + 32 * i, ch = tid & 7;
    const u32x4 v = *(const u32x4*)(t + drow * 72 + ch * 8);
    *(u32x4*)(uT + (size_t)(dtile * 64 + drow) * L + lt * 64 + ch * 8) = v;
  }
}

DI void role_bmid(int bid, const u16* __restrict__ x, const u16* __restrict__ wqkv, const u16* __restrict__ wphi,
                  char* ws, char* lds) {
  const int h = bid & 15, chunk = bid >> 4;
  const int tid = threadIdx.x, lane = tid & 63, wv = tid >> 6;
  const int r = lane & 15, kq = lane >> 4;
  const int row0 = chunk * 128 + wv * 32;
  u32x4 xf[14][2];
#pragma unroll
  for (int t = 0; t < 14; ++t)
#pragma unroll
    for (int rt = 0; rt < 2; ++rt)
      xf[t][rt] = *(const u32x4*)(x + (size_t)(row0 + 16 * rt + r) * D + 32 * t + 8 * kq);
  u16* kvb = (u16*)(lds + 448 * 128);
  {
    const float* src = (const float*)(ws + WS_KVP) + (size_t)h * 16 * 2048 + tid * 8;
    f32x4 s0 = {0, 0, 0, 0}, s1 = {0, 0, 0, 0};
#pragma unroll
    for (int c = 0; c < 16; ++c) {
      s0 += *(const f32x4*)(src + c * 2048);
      s1 += *(const f32x4*)(src + c * 2048 + 4);
    }
    *(u32x4*)(kvb + tid * 8) = pack8(s0, s1);
  }
  stage_w28<1>(wqkv, 3 * D, DH * h, 0, lds);
  const u16* wphi_h = wphi + h * DH * DH;
  const u32x4 wp0 = wphi_frag(wphi_h, lane, 0), wp1 = wphi_frag(wphi_h, lane, 1);
  __syncthreads();
  u32x4 kva[2][2];
#pragma unroll
  for (int ks = 0; ks < 2; ++ks)
#pragma unroll
    for (int dt = 0; dt < 2; ++dt) {
      unsigned e[8];
#pragma unroll
      for (int j = 0; j < 8; ++j) {
        const int f = (j < 4) ? (4 * kq + j) : (16 + 4 * kq + j - 4);
        e[j] = kvb[(32 * ks + f) * 32 + 16 * dt + r];
      }
      kva[ks][dt] = u32x4{e[0] | (e[1] << 16), e[2] | (e[3] << 16), e[4] | (e[5] << 16), e[6] | (e[7] << 16)};
    }
  const WFragAddr wa = wfrag_addr(lane);
  f32x4 qT[2][2];
#pragma unroll
  for (int i = 0; i < 2; ++i)
#pragma unroll
    for (int j = 0; j < 2; ++j) qT[i][j] = f32x4{0, 0, 0, 0};
#pragma unroll
  for (int t = 0; t < 14; ++t) {
#pragma unroll
    for (int ct = 0; ct < 2; ++ct) {
      const u32x4 wq = wfrag(lds, wa, ct, t);
#pragma unroll
      for (int rt = 0; rt < 2; ++rt) qT[ct][rt] = mfma16(wq, xf[t][rt], qT[ct][rt]);
    }
  }
  u16* o = (u16*)(ws + WS_O);
#pragma unroll
  for (int rt = 0; rt < 2; ++rt) {
    const u32x4 qb = pack8(qT[0][rt], qT[1][rt]);
    f32x4 p0 = mfma16(wp0, qb, f32x4{0, 0, 0, 0});
    f32x4 p1 = mfma16(wp1, qb, f32x4{0, 0, 0, 0});
    const bool valid1 = kq < 3;
    float a[4], b[4];
    float mx = -INFINITY, mn = INFINITY;
#pragma unroll
    for (int g = 0; g < 4; ++g) {
      a[g] = rbf(p0[g]); b[g] = rbf(p1[g]);
      mx = fmaxf(mx, a[g]); mn = fminf(mn, a[g]);
      if (valid1) { mx = fmaxf(mx, b[g]); mn = fminf(mn, b[g]); }
    }
    mx = fmaxf(mx, __shfl_xor(mx, 16)); mx = fmaxf(mx, __shfl_xor(mx, 32));
    mn = fminf(mn, __shfl_xor(mn, 16)); mn = fminf(mn, __shfl_xor(mn, 32));
    float ea[4], eb[4], na[4], nb[4], sp = 0.f, sn = 0.f;
#pragma unroll
    for (int g = 0; g < 4; ++g) {
      ea[g] = __expf(a[g] - mx); eb[g] = valid1 ? __expf(b[g] - mx) : 0.f;
      na[g] = __expf(mn - a[g]); nb[g] = valid1 ? __expf(mn - b[g]) : 0.f;
      sp += ea[g] + eb[g]; sn += na[g] + nb[g];
    }
    sp += __shfl_xor(sp, 16); sp += __shfl_xor(sp, 32);
    sn += __shfl_xor(sn, 16); sn += __shfl_xor(sn, 32);
    const float ip = 1.f / sp, in = 1.f / sn;
    const u32x4 bp = {pack2(ea[0] * ip, ea[1] * ip), pack2(ea[2] * ip, ea[3] * ip), pack2(eb[0] * ip, eb[1] * ip), pack2(eb[2] * ip, eb[3] * ip)};
    const u32x4 bn = {pack2(na[0] * in, na[1] * in), pack2(na[2] * in, na[3] * in), pack2(nb[0] * in, nb[1] * in), pack2(nb[2] * in, nb[3] * in)};
    f32x4 n0 = mfma16(kva[0][0], bp, f32x4{0, 0, 0, 0});
    n0 = mfma16(kva[1][0], bn, n0);
    f32x4 n1 = mfma16(kva[0][1], bp, f32x4{0, 0, 0, 0});
    n1 = mfma16(kva[1][1], bn, n1);
    float den = __shfl(n1[0], 48 + r);
    den = rbf(rbf(den) + 1e-6f);
    u16* orow = o + (size_t)(row0 + 16 * rt + r) * D + h * DH + 4 * kq;
    {
      u32x2 s = {pack2(rbf(n0[0]) / den, rbf(n0[1]) / den), pack2(rbf(n0[2]) / den, rbf(n0[3]) / den)};
      *(u32x2*)(orow) = s;
      if (kq < 3) {
        u32x2 s2 = {pack2(rbf(n1[0]) / den, rbf(n1[1]) / den), pack2(rbf(n1[2]) / den, rbf(n1[3]) / den)};
        *(u32x2*)(orow + 16) = s2;
      }
    }
  }
}

constexpr int UC_STRIDE = 4416;
constexpr int CV_KREV = 8 * UC_STRIDE;
DI void role_conv(int d, char* ws, char* lds) {
  const int tid = threadIdx.x, lane = tid & 63, wv = tid >> 6;
  const u16* uT = (const u16*)(ws + WS_UT) + (size_t)d * L;
  {
    const u32x4* src = (const u32x4*)(ws + WS_KREV + (size_t)d * 2 * KREV_LEN * 2);
    u32x4 v0 = src[tid], v1 = src[tid + 256], v2 = {0, 0, 0, 0};
    if (tid < 580 - 512) v2 = src[tid + 512];
    const u32x4 a = *(const u32x4*)(uT + tid * 8);
    u32x4 b = {0, 0, 0, 0};
    if (tid < 255) b = *(const u32x4*)(uT + tid * 8 + 8);
    *(u32x4*)(lds + CV_KREV + tid * 16) = v0;
    *(u32x4*)(lds + CV_KREV + (tid + 256) * 16) = v1;
    if (tid < 580 - 512) *(u32x4*)(lds + CV_KREV + (tid + 512) * 16) = v2;
    unsigned w[8] = {a[0], a[1], a[2], a[3], b[0], b[1], b[2], b[3]};
#pragma unroll
    for (int s = 0; s < 8; ++s) {
      u32x4 o;
#pragma unroll
      for (int j = 0; j < 4; ++j) {
        const int k = (s >> 1) + j;
        o[j] = (s & 1) ? __builtin_amdgcn_alignbyte(w[k + 1 < 8 ? k + 1 : 7], w[k], 2) : w[k];
      }
      *(u32x4*)(lds + s * UC_STRIDE + 64 + tid * 16) = o;
    }
    if (tid < 8) {
      const int s = tid;
      u16* c = (u16*)(lds + s * UC_STRIDE);
      for (int i = 0; i < 32; ++i) c[i] = (i - 32 + s >= 0) ? uT[i - 32 + s] : (u16)0;
      for (int i = 32 + L; i < 2112; ++i) c[i] = 0;
    }
  }
  __syncthreads();
  const int c = lane & 15, kq = lane >> 4, r = c;
  const char* abase = lds + (r & 7) * UC_STRIDE + (32 + 8 * kq + (r & 8)) * 2;
  const char* bbase = lds + CV_KREV + (KOFF + 8 * kq - 16 * c) * 2;
  f32x4 acc[8];
#pragma unroll
  for (int i = 0; i < 8; ++i) acc[i] = f32x4{0, 0, 0, 0};
  for (int s = wv; s < 65; s += 4) {
    const int o = -1824 + 32 * s;
    const u32x4 bh = *(const u32x4*)(bbase + o * 2);
    const u32x4 bl = *(const u32x4*)(bbase + o * 2 + KREV_LEN * 2);
    const int ptmin = (56 - s + 7) >> 3;
#pragma unroll
    for (int pt = 7; pt >= 0; --pt) {
      if (pt >= ptmin) {
        const u32x4 a = *(const u32x4*)(abase + (o + 256 * pt) * 2);
        acc[pt] = mfma16(a, bh, acc[pt]);
        acc[pt] = mfma16(a, bl, acc[pt]);
      }
    }
  }
  __syncthreads();
  float* red = (float*)lds;
#pragma unroll
  for (int pt = 0; pt < 8; ++pt) *(f32x4*)(red + wv * 2048 + 256 * pt + 16 * c + 4 * kq) = acc[pt];
  __syncthreads();
  {
    f32x4 s0 = *(const f32x4*)(red + tid * 8), s1 = *(const f32x4*)(red + tid * 8 + 4);
#pragma unroll
    for (int w2 = 1; w2 < 4; ++w2) {
      s0 += *(const f32x4*)(red + w2 * 2048 + tid * 8);
      s1 += *(const f32x4*)(red + w2 * 2048 + tid * 8 + 4);
    }
    u16* yT = (u16*)(ws + WS_YT) + (size_t)d * L;
    *(u32x4*)(yT + tid * 8) = pack8(s0, s1);
  }
}

DI void role_bout(int bid, const u16* __restrict__ wo, u16* __restrict__ out, char* ws, char* lds) {
  const int nt = bid % 7, mt = bid / 7;
  const int tid = threadIdx.x, lane = tid & 63, wv = tid >> 6;
  const int r = lane & 15, kq = lane >> 4;
  const u16* o = (const u16*)(ws + WS_O);
  const int row = mt * 64 + wv * 16 + r;
  u32x4 of[14];
#pragma unroll
  for (int t = 0; t < 14; ++t) of[t] = *(const u32x4*)(o + (size_t)row * D + 32 * t + 8 * kq);
  {
    u32x4 v[14];
#pragma unroll
    for (int it = 0; it < 14; ++it) {
      const int idx = tid + 256 * it, rw = idx >> 3, s = idx & 7;
      v[it] = *(const u32x4*)(wo + (size_t)rw * D + nt * 64 + s * 8);
    }
#pragma unroll
    for (int it = 0; it < 14; ++it) {
      const int idx = tid + 256 * it, rw = idx >> 3, s = idx & 7;
      *(u32x4*)(lds + rw * 128 + ((((unsigned)(s >> 1)) ^ wimg_g(rw)) << 5) + ((s & 1) << 4)) = v[it];
    }
  }
  __syncthreads();
  const int q4 = (lane & 15) >> 2, p = lane & 3;
  const unsigned g = (unsigned)((q4 >> 1) | ((kq & 1) << 1));
  f32x4 acc[4];
#pragma unroll
  for (int i = 0; i < 4; ++i) acc[i] = f32x4{0, 0, 0, 0};
  unsigned aoff[4];
#pragma unroll
  for (int ct = 0; ct < 4; ++ct) {
    const int col = 32 * (ct >> 1) + 8 * p + 4 * (ct & 1);
    const unsigned chunk = (unsigned)(col >> 4);
    aoff[ct] = (unsigned)((8 * kq + q4) * 128) + ((chunk ^ g) << 5) + (unsigned)((col & 15) * 2);
  }
#pragma unroll
  for (int t = 0; t < 14; ++t) {
#pragma unroll
    for (int ct = 0; ct < 4; ++ct) {
      const u32x2 lo = tr_read(lds, aoff[ct] + t * 4096u), hi = tr_read(lds, aoff[ct] + t * 4096u + 512u);
      acc[ct] = mfma16(u32x4{lo[0], lo[1], hi[0], hi[1]}, of[t], acc[ct]);
    }
  }
  u16* orow = out + (size_t)row * OUTW + D + nt * 64 + 8 * kq;
  *(u32x4*)(orow) = pack8(acc[0], acc[1]);
  *(u32x4*)(orow + 32) = pack8(acc[2], acc[3]);
}

DI void role_aout(int tile, const u16* __restrict__ u, const u16* __restrict__ dskip, u16* __restrict__ out, char* ws, char* lds) {
  const int lt = tile / 7, dtile = tile % 7;
  const int tid = threadIdx.x;
  u16* t = (u16*)lds;
  const u16* yT = (const u16*)(ws + WS_YT);
  u32x4 uv[2];
#pragma unroll
  for (int i = 0; i < 2; ++i) {
    const int row = (tid >> 3) + 32 * i, ch = tid & 7;
    uv[i] = *(const u32x4*)(u + (size_t)(lt * 64 + row) * D + dtile * 64 + ch * 8);
  }
  const u32x4 dk = *(const u32x4*)(dskip + dtile * 64 + (tid & 7) * 8);
#pragma unroll
  for (int i = 0; i < 2; ++i) {
    const int drow = (tid >> 3) + 32 * i, ch = tid & 7;
    const u32x4 v = *(const u32x4*)(yT + (size_t)(dtile * 64 + drow) * L + lt * 64 + ch * 8);
#pragma unroll
    for (int j = 0; j < 4; ++j) {
      t[(ch * 8 + 2 * j) * 72 + drow] = (u16)(v[j] & 0xffffu);
      t[(ch * 8 + 2 * j + 1) * 72 + drow] = (u16)(v[j] >> 16);
    }
  }
  __syncthreads();
#pragma unroll
  for (int i = 0; i < 2; ++i) {
    const int row = (tid >> 3) + 32 * i, ch = tid & 7;
    const u32x4 y = *(const u32x4*)(t + row * 72 + ch * 8);
    u32x4 o;
#pragma unroll
    for (int j = 0; j < 4; ++j) {
      const float y0 = __uint_as_float(y[j] << 16), y1 = __uint_as_float(y[j] & 0xffff0000u);
      const float u0 = __uint_as_float(uv[i][j] << 16), u1 = __uint_as_float(uv[i][j] & 0xffff0000u);
      const float d0 = __uint_as_float(dk[j] << 16), d1 = __uint_as_float(dk[j] & 0xffff0000u);
      o[j] = pack2(y0 + rbf(u0 * d0), y1 + rbf(u1 * d1));
    }
    *(u32x4*)(out + (size_t)(lt * 64 + row) * OUTW + dtile * 64 + ch * 8) = o;
  }
}

constexpr int LDS1 = 4 * KG_WAVE;
constexpr int LDS2 = 448 * 128 + 4096;
constexpr int LDS3 = 448 * 128;

__global__ __launch_bounds__(256, 2) void k_l1(const u16* u, const u16* logAre, const u16* Aim, const u16* Cre, const u16* Cim,
                                               const u16* logdt, const u16* x, const u16* wqkv, const u16* wphi, char* ws) {
  __shared__ __attribute__((aligned(16))) char lds[LDS1];
  const int b = blockIdx.x;
  if (b < 448) role_kgen(b, logAre, Aim, Cre, Cim, logdt, ws, lds);
  else if (b < 448 + 256) role_b1(b - 448, x, wqkv, wphi, ws, lds);
  else role_utrans(b - 704, u, ws, lds);
}
__global__ __launch_bounds__(256, 2) void k_l2(const u16* x, const u16* wqkv, const u16* wphi, char* ws) {
  __shared__ __attribute__((aligned(16))) char lds[LDS2];
  const int b = blockIdx.x;
  if (b < 448) role_conv(b, ws, lds);
  else role_bmid(b - 448, x, wqkv, wphi, ws, lds);
}
__global__ __launch_bounds__(256, 2) void k_l3(const u16* u, const u16* dskip, const u16* wo, u16* out, char* ws) {
  __shared__ __attribute__((aligned(16))) char lds[LDS3];
  const int b = blockIdx.x;
  if (b < 224) role_bout(b, wo, out, ws, lds);
  else role_aout(b - 224, u, dskip, out, ws, lds);
}

extern "C" void kernel_launch(void* const* d_in, const int* in_sizes, int n_in, void* d_out, int out_size, void* d_ws,
                              size_t ws_size, hipStream_t stream) {
  const u16* u = (const u16*)d_in[0];
  const u16* logAre = (const u16*)d_in[1];
  const u16* Aim = (const u16*)d_in[2];
  const u16* Cre = (const u16*)d_in[3];
  const u16* Cim = (const u16*)d_in[4];
  const u16* logdt = (const u16*)d_in[5];
  const u16* dskip = (const u16*)d_in[6];
  const u16* x = (const u16*)d_in[7];
  const u16* wqkv = (const u16*)d_in[8];
  const u16* wphi = (const u16*)d_in[9];
  const u16* wo = (const u16*)d_in[10];
  u16* out = (u16*)d_out;
  char* ws = (char*)d_ws;
  k_l1<<<448 + 256 + 224, 256, 0, stream>>>(u, logAre, Aim, Cre, Cim, logdt, x, wqkv, wphi, ws);
  k_l2<<<448 + 256, 256, 0, stream>>>(x, wqkv, wphi, ws);
  k_l3<<<224 + 224, 256, 0, stream>>>(u, dskip, wo, out, ws);
}
```

```cpp
#include <hip/hip_runtime.h>
#include <hip/hip_bf16.h>
#include <stdint.h>

typedef unsigned short u16;
typedef __bf16 bf16x8 __attribute__((ext_vector_type(8)));
typedef float f32x4 __attribute__((ext_vector_type(4)));
typedef short s16x4 __attribute__((ext_vector_type(4)));
typedef unsigned u32x4 __attribute__((ext_vector_type(4)));
typedef unsigned u32x2 __attribute__((ext_vector_type(2)));
#define LDS_AS __attribute__((address_space(3)))
#define DI __device__ __forceinline__
#if defined(__HIP_DEVICE_COMPILE__) && !defined(ALLOW_PK)
#define NOPK __attribute__((target("no-packed-fp32-ops")))
#else
#define NOPK
#endif
#define CFENCE() asm volatile("" ::: "memory")

constexpr int L = 2048, D = 448, NST = 256, H = 16, DH = 28;
constexpr int OUTW = 2 * D;

constexpr int KREV_LEN = 2320;
constexpr int KOFF = 2064;
constexpr size_t WS_UT = 0;
constexpr size_t WS_YT = WS_UT + (size_t)D * L * 2;
constexpr size_t WS_KREV = WS_YT + (size_t)D * L * 2;
constexpr size_t WS_KVP = WS_KREV + (size_t)D * 2 * KREV_LEN * 2;
constexpr size_t WS_O = WS_KVP + (size_t)H * 16 * 2048 * 4;
constexpr size_t WS_QF = WS_O + (size_t)L * D * 2;
constexpr size_t WS_END = WS_QF + (size_t)H * 128 * 1024;

#define STAMP(i)
#define STAMPR(role, i)
DI float bf2f(u16 v) { return __uint_as_float(((unsigned)v) << 16); }
DI u16 f2bf(float f) { __bf16 b = (__bf16)f; return __builtin_bit_cast(u16, b); }
DI float rbf(float f) { return bf2f(f2bf(f)); }
typedef __bf16 bf16x2 __attribute__((ext_vector_type(2)));
typedef float f32x2 __attribute__((ext_vector_type(2)));
DI unsigned pack2(float a, float b) {
  f32x2 v = {a, b};
  return __builtin_bit_cast(unsigned, __builtin_convertvector(v, bf16x2));
}
DI float fdiv(float n, float d, float rd) { const float q = n * rd; return fmaf(fmaf(-q, d, n), rd, q); }
DI f32x4 mfma16(u32x4 a, u32x4 b, f32x4 c) {
  return __builtin_amdgcn_mfma_f32_16x16x32_bf16(__builtin_bit_cast(bf16x8, a), __builtin_bit_cast(bf16x8, b), c, 0, 0, 0);
}
template <int CTRL> DI float dppf(float x) {
  return __builtin_bit_cast(float, __builtin_amdgcn_update_dpp(0, __builtin_bit_cast(int, x), CTRL, 0xf, 0xf, false));
}
DI float row16_max(float x) {
  x = fmaxf(x, dppf<0x128>(x)); x = fmaxf(x, dppf<0x124>(x)); x = fmaxf(x, dppf<0x122>(x)); x = fmaxf(x, dppf<0x121>(x)); return x;
}
DI float row16_min(float x) {
  x = fminf(x, dppf<0x128>(x)); x = fminf(x, dppf<0x124>(x)); x = fminf(x, dppf<0x122>(x)); x = fminf(x, dppf<0x121>(x)); return x;
}
DI float row16_sum(float x) {
  x += dppf<0x128>(x); x += dppf<0x124>(x); x += dppf<0x122>(x); x += dppf<0x121>(x); return x;
}
DI u32x2 tr_read(const char* lds, unsigned off) {
  s16x4 v = __builtin_amdgcn_ds_read_tr16_b64_v4i16((LDS_AS s16x4*)(lds + off));
  return __builtin_bit_cast(u32x2, v);
}

DI unsigned wimg_g(unsigned row) { return ((row >> 1) & 1u) | (((row >> 3) & 1u) << 1); }

template <int NPARTS>
DI void stage_w28(const u16* __restrict__ w, int ldw, int col0, int col1, char* lds) {
  const int tid = threadIdx.x;
  constexpr int SL = NPARTS * 8;
  constexpr int TOT = 448 * SL;
  constexpr int IT = TOT / 256;
  u32x2 v[IT];
#pragma unroll
  for (int it = 0; it < IT; ++it) {
    const int idx = tid + 256 * it;
    const int row = idx / SL, s = idx % SL;
    const int part = s >> 3, i = s & 7;
    u32x2 t = {0u, 0u};
    if (i < 7) t = *(const u32x2*)(w + (size_t)row * ldw + (part ? col1 : col0) + 4 * i);
    v[it] = t;
  }
#pragma unroll
  for (int it = 0; it < IT; ++it) {
    const int idx = tid + 256 * it;
    const int row = idx / SL, s = idx % SL;
    const unsigned chunk = (unsigned)(s >> 2);
    *(u32x2*)(lds + row * 128 + ((chunk ^ wimg_g(row)) << 5) + ((s & 3) << 3)) = v[it];
  }
}

struct WFragAddr { unsigned base; unsigned g; };
DI WFragAddr wfrag_addr(int lane) {
  const int kq = lane >> 4, q4 = (lane & 15) >> 2, p = lane & 3;
  WFragAddr a;
  a.base = (unsigned)((8 * kq + q4) * 128 + p * 8);
  a.g = (unsigned)((q4 >> 1) | ((kq & 1) << 1));
  return a;
}
DI u32x4 wfrag(const char* lds, const WFragAddr& a, int ct, int t) {
  const unsigned o = a.base + (((unsigned)ct ^ a.g) << 5) + (unsigned)t * 4096u;
  u32x2 lo = tr_read(lds, o), hi = tr_read(lds, o + 512u);
  u32x4 r = {lo[0], lo[1], hi[0], hi[1]};
  return r;
}

DI u32x4 wphi_frag(const u16* __restrict__ wphi_h, int lane, int ft) {
  const int f = (lane & 15) + 16 * ft, kq = lane >> 4;
  unsigned e[8];
#pragma unroll
  for (int j = 0; j < 8; ++j) {
    const int d = (j < 4) ? (4 * kq + j) : (16 + 4 * kq + j - 4);
    e[j] = (d < DH && f < DH) ? (unsigned)wphi_h[d * DH + f] : 0u;
  }
  u32x4 r = {e[0] | (e[1] << 16), e[2] | (e[3] << 16), e[4] | (e[5] << 16), e[6] | (e[7] << 16)};
  return r;
}
DI u32x4 pack8(const f32x4& a, const f32x4& b) {
  u32x4 r = {pack2(a[0], a[1]), pack2(a[2], a[3]), pack2(b[0], b[1]), pack2(b[2], b[3])};
  return r;
}

constexpr int B1_ROWB = 192;
constexpr int B1_XOFF = 64 * B1_ROWB;
struct B1Stage { u32x2 w[8]; u32x4 x[4]; };
DI void b1_load(const u16* __restrict__ wsrc, const u16* __restrict__ xsrc, int c, B1Stage& st) {
  if (threadIdx.x < 192) {
#pragma unroll
    for (int it = 0; it < 8; ++it) st.w[it] = *(const u32x2*)(wsrc + (size_t)(c * 64 + it * 8) * (3 * D));
  }
#pragma unroll
  for (int it = 0; it < 4; ++it) st.x[it] = *(const u32x4*)(xsrc + (size_t)it * 32 * D + c * 64);
}
DI void b1_store(char* wd0, char* wd1, bool wz, char* xd, const B1Stage& st) {
  if (threadIdx.x < 192) {
#pragma unroll
    for (int it = 0; it < 8; ++it) *(u32x2*)(((it & 1) ? wd1 : wd0) + it * 8 * B1_ROWB) = wz ? u32x2{0u, 0u} : st.w[it];
  }
#pragma unroll
  for (int it = 0; it < 4; ++it) *(u32x4*)(xd + it * 32 * 128) = st.x[it];
}
DI u32x4 wphi_frag2(const u16* __restrict__ wphi_h, int lane, int ft) {
  const int f = (lane & 15) + 16 * ft, kq = lane >> 4;
  const int fc = f < DH ? f : DH - 1;
  unsigned e[8];
#pragma unroll
  for (int j = 0; j < 8; ++j) {
    const int d = (j < 4) ? (4 * kq + j) : (16 + 4 * kq + j - 4);
    const unsigned v = wphi_h[(d < DH ? d : DH - 1) * DH + fc];
    e[j] = (d < DH && f < DH) ? v : 0u;
  }
  u32x4 r = {e[0] | (e[1] << 16), e[2] | (e[3] << 16), e[4] | (e[5] << 16), e[6] | (e[7] << 16)};
  return r;
}

DI float xmax(float x) {
  auto r = __builtin_amdgcn_permlane16_swap(__float_as_uint(x), __float_as_uint(x), false, false);
  x = fmaxf(__uint_as_float(r[0]), __uint_as_float(r[1]));
  auto r2 = __builtin_amdgcn_permlane32_swap(__float_as_uint(x), __float_as_uint(x), false, false);
  return fmaxf(__uint_as_float(r2[0]), __uint_as_float(r2[1]));
}
DI float xsum(float x) {
  auto r = __builtin_amdgcn_permlane16_swap(__float_as_uint(x), __float_as_uint(x), false, false);
  x = __uint_as_float(r[0]) + __uint_as_float(r[1]);
  auto r2 = __builtin_amdgcn_permlane32_swap(__float_as_uint(x), __float_as_uint(x), false, false);
  return __uint_as_float(r2[0]) + __uint_as_float(r2[1]);
}
DI void phi_rows(const u32x4& wp0, const u32x4& wp1, const u32x4& xb, bool valid1, u32x4& bp, u32x4& bn) {
  const f32x4 p0 = mfma16(wp0, xb, f32x4{0, 0, 0, 0});
  const f32x4 p1 = mfma16(wp1, xb, f32x4{0, 0, 0, 0});
  float a[4], b[4];
#pragma unroll
  for (int g = 0; g < 4; ++g) { a[g] = rbf(p0[g]); b[g] = rbf(p1[g]); b[g] = valid1 ? b[g] : a[g]; }
  float mx = fmaxf(fmaxf(fmaxf(a[0], a[1]), fmaxf(a[2], a[3])), fmaxf(fmaxf(b[0], b[1]), fmaxf(b[2], b[3])));
  float mn = fminf(fminf(fminf(a[0], a[1]), fminf(a[2], a[3])), fminf(fminf(b[0], b[1]), fminf(b[2], b[3])));
  mx = xmax(mx); mn = -xmax(-mn);
  float ea[4], eb[4], na[4], nb[4];
#pragma unroll
  for (int g = 0; g < 4; ++g) {
    ea[g] = __expf(a[g] - mx); eb[g] = valid1 ? __expf(b[g] - mx) : 0.f;
    na[g] = __expf(mn - a[g]); nb[g] = valid1 ? __expf(mn - b[g]) : 0.f;
  }
  const float sp = xsum(((ea[0] + ea[1]) + (ea[2] + ea[3])) + ((eb[0] + eb[1]) + (eb[2] + eb[3])));
  const float sn = xsum(((na[0] + na[1]) + (na[2] + na[3])) + ((nb[0] + nb[1]) + (nb[2] + nb[3])));
  const float ip = __builtin_amdgcn_rcpf(sp), in = __builtin_amdgcn_rcpf(sn);
  bp = u32x4{pack2(ea[0] * ip, ea[1] * ip), pack2(ea[2] * ip, ea[3] * ip), pack2(eb[0] * ip, eb[1] * ip), pack2(eb[2] * ip, eb[3] * ip)};
  bn = u32x4{pack2(na[0] * in, na[1] * in), pack2(na[2] * in, na[3] * in), pack2(nb[0] * in, nb[1] * in), pack2(nb[2] * in, nb[3] * in)};
}

DI void role_b1(int bid, const u16* __restrict__ x, const u16* __restrict__ wqkv, const u16* __restrict__ wphi,
                char* ws, char* lds) {
  const int h = bid >> 4, chunk = ((bid & 7) << 1) | ((bid >> 3) & 1);
  const int tid = threadIdx.x, lane = tid & 63, wv = tid >> 6;
  const int r = lane & 15, kq = lane >> 4;
  const int row0 = chunk * 128 + wv * 32;
  STAMPR(2, 0);
  const int srow = tid / 24, ss = tid % 24;
  const u16* wsrc = wqkv + (size_t)srow * (3 * D) + (ss >> 3) * D + DH * h + 4 * ((ss & 7) < 7 ? (ss & 7) : 6);
  char* wd0 = lds + srow * B1_ROWB + ((ss >> 2) << 5) + ((ss & 3) << 3);
  char* wd1 = lds + srow * B1_ROWB + (((ss >> 2) ^ 1) << 5) + ((ss & 3) << 3);
  const bool wz = (ss & 7) == 7;
  const u16* xsrc = x + (size_t)(chunk * 128 + (tid >> 3)) * D + (tid & 7) * 8;
  char* xd = lds + B1_XOFF + (tid >> 3) * 128 + (((tid & 7) ^ ((tid >> 3) & 7)) << 4);
  B1Stage st;
  b1_load(wsrc, xsrc, 0, st);
  b1_store(wd0, wd1, wz, xd, st);
  b1_load(wsrc, xsrc, 1, st);
  STAMPR(2, 1);
  __syncthreads();
  STAMPR(2, 2);
  const int q4 = (lane & 15) >> 2, p = lane & 3;
  const unsigned wbase = (unsigned)((8 * kq + q4) * B1_ROWB + p * 8);
  const unsigned gsw = (unsigned)(kq & 1);
  const char* xr0 = lds + B1_XOFF + (wv * 32 + r) * 128 + ((kq ^ (r & 7)) << 4);
  const char* xr1 = lds + B1_XOFF + (wv * 32 + r) * 128 + (((4 + kq) ^ (r & 7)) << 4);
  f32x4 qT[2][2], kT[2][2], vv[2][2];
#pragma unroll
  for (int i = 0; i < 2; ++i)
#pragma unroll
    for (int j = 0; j < 2; ++j) { qT[i][j] = f32x4{0, 0, 0, 0}; kT[i][j] = f32x4{0, 0, 0, 0}; vv[i][j] = f32x4{0, 0, 0, 0}; }
  auto compute = [&]() {
#pragma unroll
    for (int tt = 0; tt < 2; ++tt) {
      u32x4 xf[2];
#pragma unroll
      for (int rt = 0; rt < 2; ++rt) xf[rt] = *(const u32x4*)((tt ? xr1 : xr0) + rt * 16 * 128);
#pragma unroll
      for (int ct = 0; ct < 2; ++ct) {
        u32x4 wf[3];
#pragma unroll
        for (int pt = 0; pt < 3; ++pt) {
          const unsigned o = wbase + (((unsigned)(2 * pt + ct) ^ gsw) << 5) + (unsigned)tt * (32u * B1_ROWB);
          const u32x2 lo = tr_read(lds, o), hi = tr_read(lds, o + 4u * B1_ROWB);
          wf[pt] = u32x4{lo[0], lo[1], hi[0], hi[1]};
        }
#pragma unroll
        for (int rt = 0; rt < 2; ++rt) {
          qT[ct][rt] = mfma16(wf[0], xf[rt], qT[ct][rt]);
          kT[ct][rt] = mfma16(wf[1], xf[rt], kT[ct][rt]);
          vv[rt][ct] = mfma16(xf[rt], wf[2], vv[rt][ct]);
        }
      }
    }
  };
#pragma unroll 1
  for (int c = 0; c < 6; ++c) {
    compute();
    __syncthreads();
    b1_store(wd0, wd1, wz, xd, st);
    if (c + 2 < 7) b1_load(wsrc, xsrc, c + 2, st);
    __syncthreads();
  }
  compute();
  STAMPR(2, 3);
  const u16* wphi_h = wphi + h * DH * DH;
  const u32x4 wp0 = wphi_frag2(wphi_h, lane, 0), wp1 = wphi_frag2(wphi_h, lane, 1);
  {
    u32x4* qf = (u32x4*)(ws + WS_QF) + (size_t)(h * 128 + (row0 >> 4)) * 64 + lane;
    qf[0] = pack8(qT[0][0], qT[1][0]);
    qf[64] = pack8(qT[0][1], qT[1][1]);
  }
  f32x4 tp[2][2], tn[2][2];
  {
    const unsigned one = (kq == (r >> 2)) ? ((r & 1) ? 0x3f800000u : 0x00003f80u) : 0u;
    const u32x4 id0 = {(r & 2) ? 0u : one, (r & 2) ? one : 0u, 0u, 0u};
    const u32x4 id1 = {0u, 0u, (r & 2) ? 0u : one, (r & 2) ? one : 0u};
#pragma unroll
    for (int rt = 0; rt < 2; ++rt) {
      u32x4 bp, bn;
      phi_rows(wp0, wp1, pack8(kT[0][rt], kT[1][rt]), kq < 3, bp, bn);
      tp[rt][0] = mfma16(bp, id0, f32x4{0, 0, 0, 0}); tp[rt][1] = mfma16(bp, id1, f32x4{0, 0, 0, 0});
      tn[rt][0] = mfma16(bn, id0, f32x4{0, 0, 0, 0}); tn[rt][1] = mfma16(bn, id1, f32x4{0, 0, 0, 0});
    }
  }
  u32x4 vb[2];
#pragma unroll
  for (int dt = 0; dt < 2; ++dt) vb[dt] = pack8(vv[0][dt], vv[1][dt]);
  if (r == DH - 16) vb[1] = u32x4{0x3f803f80u, 0x3f803f80u, 0x3f803f80u, 0x3f803f80u};
  f32x4 kv[4][2];
#pragma unroll
  for (int ft = 0; ft < 2; ++ft) {
    const u32x4 ap = pack8(tp[0][ft], tp[1][ft]);
    const u32x4 an = pack8(tn[0][ft], tn[1][ft]);
#pragma unroll
    for (int dt = 0; dt < 2; ++dt) {
      kv[ft][dt] = mfma16(ap, vb[dt], f32x4{0, 0, 0, 0});
      kv[2 + ft][dt] = mfma16(an, vb[dt], f32x4{0, 0, 0, 0});
    }
  }
  STAMPR(2, 4);
  __syncthreads();
  float* red = (float*)lds;
#pragma unroll
  for (int f4 = 0; f4 < 4; ++f4)
#pragma unroll
    for (int dt = 0; dt < 2; ++dt)
#pragma unroll
      for (int g = 0; g < 4; ++g)
        red[wv * 2048 + (16 * f4 + 4 * kq + g) * 32 + 16 * dt + r] = kv[f4][dt][g];
  __syncthreads();
  float* dst = (float*)(ws + WS_KVP) + (size_t)(h * 16 + chunk) * 2048;
#pragma unroll
  for (int i = 0; i < 2; ++i) {
    const int e = (tid + 256 * i) * 4;
    f32x4 s = *(const f32x4*)(red + e);
    s += *(const f32x4*)(red + 2048 + e);
    s += *(const f32x4*)(red + 4096 + e);
    s += *(const f32x4*)(red + 6144 + e);
    *(f32x4*)(dst + e) = s;
  }
  STAMPR(2, 5);
}

struct cplx { float re, im; };
DI cplx cmul(cplx a, cplx b) { return cplx{a.re * b.re - a.im * b.im, a.re * b.im + a.im * b.re}; }

constexpr int KG_STRIDE = 544;
constexpr int KG_WAVE = 16 * KG_STRIDE;

DI u32x2 split_entry(float re, float im) {
  const unsigned h = pack2(re, im);
  const float rr = re - __uint_as_float(h << 16), ri = im - __uint_as_float(h & 0xffff0000u);
  u32x2 o = {h, pack2(rr, ri)};
  return o;
}

constexpr int kpos_index(int l) {
  int pos = l;
  if (l >= 256) {
    const int sh = (l >= 1024) ? 3 : (l >= 512 ? 2 : 1);
    const int half = 1 << (sh - 1);
    const int lsb = (l >> sh) & 1;
    pos = ((l + half - 1 + lsb) >> sh) << sh;
  }
  int row = 40;
  const int j = (pos >> 3) & 15;
  if (pos < 256) row = ((pos >> 7) << 3) + (pos & 7);
  else if (pos < 512) row = 16 + (((pos >> 7) & 1) << 2) + ((pos >> 1) & 3);
  else if (pos < 1024) row = 24 + (((pos >> 7) & 3) << 1) + ((pos >> 2) & 1);
  else if (pos < 2048) row = 32 + ((pos >> 7) & 7);
  return row * 16 + j;
}
struct KIdxTab {
  alignas(16) u16 v[KREV_LEN];
  constexpr KIdxTab() : v() {
    for (int i = 0; i < KREV_LEN; ++i) {
      const int l = KOFF - i;
      v[i] = (u16)(4 * ((l >= 0 && l < L) ? kpos_index(l) : 767));
    }
  }
};
__device__ const KIdxTab KIDX = KIdxTab();

DI void role_kgen(int d, const u16* __restrict__ logAre, const u16* __restrict__ Aim, const u16* __restrict__ Cre,
                  const u16* __restrict__ Cim, const u16* __restrict__ logdt, char* ws, char* lds) {
  const int tid = threadIdx.x, lane = tid & 63, wv = tid >> 6;
  const int n = tid;
  STAMPR(1, 0);
  const float dt = rbf(expf(bf2f(logdt[d])));
  const float are = -rbf(expf(bf2f(logAre[d * NST + n])));
  const float ar = are * dt;
  const float ai = bf2f(Aim[d * NST + n]) * dt;
  const cplx C2 = {2.f * (bf2f(Cre[d * NST + n]) * dt), 2.f * (bf2f(Cim[d * NST + n]) * dt)};
  const u32x4 kidx = *(const u32x4*)(KIDX.v + (tid + 2) * 8);
  const float L2E = 1.44269502162933349609f, L2E_LO = 1.92596299112661746e-8f;
  const float I2P = 0.15915493667125701904f, I2P_LO = 6.4206383167e-9f;
  const float LN2 = 0.69314718055994530942f;
  const float xh = ar * L2E;
  const float xl = fmaf(ar, L2E, -xh) + ar * L2E_LO;
  const float th = ai * I2P;
  const float tl = fmaf(ai, I2P, -th) + ai * I2P_LO;
  auto cexp = [&](float m) -> cplx {
    const float mag = __builtin_amdgcn_exp2f(xh * m) * fmaf(xl * m, LN2, 1.f);
    const float T = th * m;
    const float rr = (T - __builtin_rintf(T)) + tl * m;
    return cplx{mag * __builtin_amdgcn_cosf(rr), mag * __builtin_amdgcn_sinf(rr)};
  };
  STAMPR(1, 1);
  char* wl = lds + wv * KG_WAVE;
  char* wr = wl + lane * 8;
  const int fi = lane & 15, kq = lane >> 4;
  const char* fr_base = wl + fi * KG_STRIDE + kq * 16;
  u32x4 fb[8];
  {
    cplx F[16];
    F[0] = cplx{1.f, 0.f};
    F[1] = cexp(8.f); F[2] = cexp(16.f); F[4] = cexp(32.f); F[8] = cexp(64.f);
    F[3] = cmul(F[2], F[1]); F[5] = cmul(F[4], F[1]); F[6] = cmul(F[4], F[2]); F[7] = cmul(F[4], F[3]);
#pragma unroll
    for (int j = 1; j < 8; ++j) F[8 + j] = cmul(F[8], F[j]);
#pragma unroll
    for (int j = 0; j < 16; ++j) *(u32x2*)(wr + j * KG_STRIDE) = split_entry(F[j].re, F[j].im);
    CFENCE();
#pragma unroll
    for (int tp = 0; tp < 8; ++tp) fb[tp] = *(const u32x4*)(fr_base + tp * 64);
    CFENCE();
  }
  STAMPR(1, 2);
  cplx E[8];
  E[0] = cplx{1.f, 0.f};
  E[1] = cexp(1.f); E[2] = cexp(2.f); E[4] = cexp(4.f);
  E[3] = cmul(E[2], E[1]); E[5] = cmul(E[4], E[1]); E[6] = cmul(E[4], E[2]); E[7] = cmul(E[4], E[3]);
  cplx Hh[17];
  {
    const cplx X1 = cexp(128.f), X2 = cexp(256.f), X4 = cexp(512.f), X8 = cexp(1024.f), X16 = cexp(2048.f);
    Hh[0] = C2; Hh[1] = cmul(C2, X1); Hh[2] = cmul(C2, X2); Hh[3] = cmul(Hh[2], X1);
    Hh[4] = cmul(C2, X4); Hh[5] = cmul(Hh[4], X1); Hh[6] = cmul(Hh[4], X2); Hh[7] = cmul(Hh[6], X1);
    Hh[8] = cmul(C2, X8);
    Hh[9] = cmul(Hh[8], X1); Hh[10] = cmul(Hh[8], X2); Hh[11] = cmul(Hh[10], X1);
    Hh[12] = cmul(Hh[8], X4); Hh[13] = cmul(Hh[12], X1); Hh[14] = cmul(Hh[12], X2); Hh[15] = cmul(Hh[14], X1);
    Hh[16] = cmul(C2, X16);
  }
  f32x4 acc[3];
#pragma unroll
  for (int tile = 0; tile < 3; ++tile) {
#pragma unroll
    for (int rr = 0; rr < 16; ++rr) {
      cplx g;
      const int row = tile * 16 + rr;
      if (row < 16) { const int i = row >> 3, e = row & 7; g = e ? cmul(Hh[i], E[e]) : Hh[i]; }
      else if (row < 24) { const int i = (row - 16) >> 2, e = (row - 16) & 3; g = e ? cmul(Hh[2 + i], E[2 * e]) : Hh[2 + i]; }
      else if (row < 32) { const int i = (row - 24) >> 1, e = (row - 24) & 1; g = e ? cmul(Hh[4 + i], E[4]) : Hh[4 + i]; }
      else if (row < 41) g = Hh[8 + row - 32];
      else g = cplx{0.f, 0.f};
      *(u32x2*)(wr + rr * KG_STRIDE) = split_entry(g.re, -g.im);
    }
    CFENCE();
    f32x4 a0 = {0, 0, 0, 0}, a1 = {0, 0, 0, 0};
#pragma unroll
    for (int tp = 0; tp < 8; ++tp) {
      const u32x4 ga = *(const u32x4*)(fr_base + tp * 64);
      a0 = mfma16(ga, fb[tp], a0);
      a1 = mfma16(ga, u32x4{fb[tp][1], fb[tp][0], fb[tp][3], fb[tp][2]}, a1);
    }
    CFENCE();
    acc[tile] = a0 + a1;
    STAMPR(1, 3 + tile);
  }
  __syncthreads();
  STAMPR(1, 6);
  float* red = (float*)lds;
#pragma unroll
  for (int tile = 0; tile < 3; ++tile)
#pragma unroll
    for (int g = 0; g < 4; ++g) red[wv * 768 + (16 * tile + 4 * kq + g) * 16 + fi] = acc[tile][g];
  __syncthreads();
  if (tid < 192) {
    f32x4 s = *(const f32x4*)(red + tid * 4);
    s += *(const f32x4*)(red + 768 + tid * 4);
    s += *(const f32x4*)(red + 1536 + tid * 4);
    s += *(const f32x4*)(red + 2304 + tid * 4);
    *(f32x4*)(red + 3072 + tid * 4) = s;
  }
  __syncthreads();
  STAMPR(1, 7);
  const char* Kc = (const char*)(red + 3072);
  u16* khi = (u16*)(ws + WS_KREV) + (size_t)d * 2 * KREV_LEN;
  u16* klo = khi + KREV_LEN;
  {
    unsigned hh[4], ll[4];
#pragma unroll
    for (int p = 0; p < 4; ++p) {
      const float v0 = *(const float*)(Kc + (kidx[p] & 0xffffu)), v1 = *(const float*)(Kc + (kidx[p] >> 16));
      const u32x2 sp = split_entry(v0, v1);
      hh[p] = sp[0]; ll[p] = sp[1];
    }
    *(u32x4*)(khi + (tid + 2) * 8) = u32x4{hh[0], hh[1], hh[2], hh[3]};
    *(u32x4*)(klo + (tid + 2) * 8) = u32x4{ll[0], ll[1], ll[2], ll[3]};
  }
  if (tid < 34) {
    const int g8 = tid < 2 ? tid : 256 + tid;
    u32x4 h = {0, 0, 0, 0}, l = {0, 0, 0, 0};
    if (g8 == 258) { const u32x2 sp = split_entry(*(const float*)Kc, 0.f); h[0] = sp[0]; l[0] = sp[1]; }
    *(u32x4*)(khi + g8 * 8) = h;
    *(u32x4*)(klo + g8 * 8) = l;
  }
  STAMPR(1, 8);
}

DI void role_utrans(int tile, const u16* __restrict__ u, char* ws, char* lds) {
  const int lt = tile / 7, dtile = tile % 7;
  const int tid = threadIdx.x;
  u16* t = (u16*)lds;
#pragma unroll
  for (int i = 0; i < 2; ++i) {
    const int row = (tid >> 3) + 32 * i, ch = tid & 7;
    const u32x4 v = *(const u32x4*)(u + (size_t)(lt * 64 + row) * D + dtile * 64 + ch * 8);
#pragma unroll
    for (int j = 0; j < 4; ++j) {
      t[(ch * 8 + 2 * j) * 72 + row] = (u16)(v[j] & 0xffffu);
      t[(ch * 8 + 2 * j + 1) * 72 + row] = (u16)(v[j] >> 16);
    }
  }
  __syncthreads();
  u16* uT = (u16*)(ws + WS_UT);
#pragma unroll
  for (int i = 0; i < 2; ++i) {
    const int drow = (tid >> 3) + 32 * i, ch = tid & 7;
    const u32x4 v = *(const u32x4*)(t + drow * 72 + ch * 8);
    *(u32x4*)(uT + (size_t)(dtile * 64 + drow) * L + lt * 64 + ch * 8) = v;
  }
}

DI void role_bmid(int bid, const u16* __restrict__ wphi, char* ws, char* lds) {
  const int h = ((bid & 7) << 1) | ((bid >> 3) & 1), chunk = bid >> 4;
  const int tid = threadIdx.x, lane = tid & 63, wv = tid >> 6;
  const int r = lane & 15, kq = lane >> 4;
  const int rt0 = chunk * 16 + wv * 4;
  STAMPR(4, 0);
  u32x4 qb[4];
  {
    const u32x4* qf = (const u32x4*)(ws + WS_QF) + (size_t)(h * 128 + rt0) * 64 + lane;
#pragma unroll
    for (int i = 0; i < 4; ++i) qb[i] = qf[64 * i];
  }
  u16* kvb = (u16*)lds;
  {
    const float* src = (const float*)(ws + WS_KVP) + (size_t)h * 16 * 2048 + tid * 8;
    f32x4 a0[16], a1[16];
#pragma unroll
    for (int c = 0; c < 16; ++c) { a0[c] = *(const f32x4*)(src + c * 2048); a1[c] = *(const f32x4*)(src + c * 2048 + 4); }
    f32x4 s0 = a0[0], s1 = a1[0];
#pragma unroll
    for (int c = 1; c < 16; ++c) { s0 += a0[c]; s1 += a1[c]; }
    *(u32x4*)(kvb + tid * 8) = pack8(s0, s1);
  }
  const u16* wphi_h = wphi + h * DH * DH;
  const u32x4 wp0 = wphi_frag2(wphi_h, lane, 0), wp1 = wphi_frag2(wphi_h, lane, 1);
  STAMPR(4, 1);
  __syncthreads();
  STAMPR(4, 2);
  u32x4 kva[2][2];
#pragma unroll
  for (int ks = 0; ks < 2; ++ks)
#pragma unroll
    for (int dt = 0; dt < 2; ++dt) {
      unsigned e[8];
#pragma unroll
      for (int j = 0; j < 8; ++j) {
        const int f = (j < 4) ? (4 * kq + j) : (16 + 4 * kq + j - 4);
        e[j] = kvb[(32 * ks + f) * 32 + 16 * dt + r];
      }
      kva[ks][dt] = u32x4{e[0] | (e[1] << 16), e[2] | (e[3] << 16), e[4] | (e[5] << 16), e[6] | (e[7] << 16)};
    }
  u16* o = (u16*)(ws + WS_O);
  const bool valid1 = kq < 3;
  u32x2 r0[4], r1[4];
#pragma unroll
  for (int rt = 0; rt < 4; ++rt) {
    u32x4 bp, bn;
    phi_rows(wp0, wp1, qb[rt], valid1, bp, bn);
    f32x4 n0 = mfma16(kva[0][0], bp, f32x4{0, 0, 0, 0});
    n0 = mfma16(kva[1][0], bn, n0);
    f32x4 n1 = mfma16(kva[0][1], bp, f32x4{0, 0, 0, 0});
    n1 = mfma16(kva[1][1], bn, n1);
    float den = __shfl(n1[0], 48 + r);
    den = rbf(rbf(den) + 1e-6f);
    const float rd = __builtin_amdgcn_rcpf(den);
    r0[rt] = u32x2{pack2(fdiv(rbf(n0[0]), den, rd), fdiv(rbf(n0[1]), den, rd)), pack2(fdiv(rbf(n0[2]), den, rd), fdiv(rbf(n0[3]), den, rd))};
    r1[rt] = u32x2{pack2(fdiv(rbf(n1[0]), den, rd), fdiv(rbf(n1[1]), den, rd)), pack2(fdiv(rbf(n1[2]), den, rd), fdiv(rbf(n1[3]), den, rd))};
  }
  {
    const int col = h * DH + 4 * kq;
    u16* p0 = o + ((size_t)(rt0 * 14 + (col >> 5)) * 64 + ((col >> 3) & 3) * 16 + r) * 8 + (col & 4);
#pragma unroll
    for (int rt = 0; rt < 4; ++rt) *(u32x2*)(p0 + rt * 14 * 512) = r0[rt];
  }
  if (kq < 3) {
    const int col = h * DH + 16 + 4 * kq;
    u16* p1 = o + ((size_t)(rt0 * 14 + (col >> 5)) * 64 + ((col >> 3) & 3) * 16 + r) * 8 + (col & 4);
#pragma unroll
    for (int rt = 0; rt < 4; ++rt) *(u32x2*)(p1 + rt * 14 * 512) = r1[rt];
  }
  STAMPR(4, 3);
}

constexpr int UC_STRIDE = 4416;
constexpr int CV_KREV = 8 * UC_STRIDE;
DI void role_conv(int d, char* ws, char* lds) {
  const int tid = threadIdx.x, lane = tid & 63, wv = tid >> 6;
  const u16* uT = (const u16*)(ws + WS_UT) + (size_t)d * L;
  STAMPR(3, 0);
  {
    const u32x4* src = (const u32x4*)(ws + WS_KREV + (size_t)d * 2 * KREV_LEN * 2);
    u32x4 v0 = src[tid], v1 = src[tid + 256], v2 = {0, 0, 0, 0};
    if (tid < 580 - 512) v2 = src[tid + 512];
    const u32x4 a = *(const u32x4*)(uT + tid * 8);
    u32x4 b = {0, 0, 0, 0};
    if (tid < 255) b = *(const u32x4*)(uT + tid * 8 + 8);
    *(u32x4*)(lds + CV_KREV + tid * 16) = v0;
    *(u32x4*)(lds + CV_KREV + (tid + 256) * 16) = v1;
    if (tid < 580 - 512) *(u32x4*)(lds + CV_KREV + (tid + 512) * 16) = v2;
    unsigned w[8] = {a[0], a[1], a[2], a[3], b[0], b[1], b[2], b[3]};
#pragma unroll
    for (int s = 0; s < 8; ++s) {
      u32x4 o;
#pragma unroll
      for (int j = 0; j < 4; ++j) {
        const int k = (s >> 1) + j;
        o[j] = (s & 1) ? __builtin_amdgcn_alignbyte(w[k + 1 < 8 ? k + 1 : 7], w[k], 2) : w[k];
      }
      *(u32x4*)(lds + s * UC_STRIDE + 64 + tid * 16) = o;
    }
    if (tid == 0) {
      unsigned z[8] = {0u, 0u, 0u, 0u, a[0], a[1], a[2], a[3]};
#pragma unroll
      for (int sft = 0; sft < 8; ++sft) {
        u32x4 o;
#pragma unroll
        for (int j = 0; j < 4; ++j) {
          const int k = (sft >> 1) + j;
          o[j] = (sft & 1) ? __builtin_amdgcn_alignbyte(z[k + 1], z[k], 2) : z[k];
        }
        *(u32x4*)(lds + sft * UC_STRIDE + 48) = o;
      }
    }
    if (tid >= 64 && tid < 64 + 56) {
      const int i = tid - 64, sft = i & 7, ch = i >> 3;
      *(u32x4*)(lds + sft * UC_STRIDE + (ch < 3 ? ch * 16 : 4160 + (ch - 3) * 16)) = u32x4{0, 0, 0, 0};
    }
  }
  STAMPR(3, 1);
  __syncthreads();
  STAMPR(3, 2);
  const int c = lane & 15, kq = lane >> 4, r = c;
  const char* abase = lds + (r & 7) * UC_STRIDE + (8 * kq + (r & 8) + 32 * wv) * 2;
  const char* bbase = lds + CV_KREV + (8 * kq - 16 * c + 240 + 32 * wv) * 2;
  f32x4 acc[8];
#pragma unroll
  for (int i = 0; i < 8; ++i) acc[i] = f32x4{0, 0, 0, 0};
#pragma unroll
  for (int b = 0; b < 8; ++b) {
#pragma unroll
    for (int hh = 0; hh < 2; ++hh) {
      const u32x4 bh = *(const u32x4*)(bbase + (256 * b + 128 * hh) * 2);
      const u32x4 bl = *(const u32x4*)(bbase + (256 * b + 128 * hh) * 2 + KREV_LEN * 2);
#pragma unroll
      for (int pt = 7; pt >= 7 - b; --pt) {
        const u32x4 a = *(const u32x4*)(abase + (-1792 + 256 * b + 128 * hh + 256 * pt) * 2);
        acc[pt] = mfma16(a, bh, acc[pt]);
        acc[pt] = mfma16(a, bl, acc[pt]);
      }
    }
  }
  {
    const char* ab2 = abase - 64 * wv;
    const char* bb2 = bbase - 64 * wv;
    const u32x4 bh = *(const u32x4*)(bb2 + 2048 * 2);
    const u32x4 bl = *(const u32x4*)(bb2 + 2048 * 2 + KREV_LEN * 2);
    const u32x4 a0 = *(const u32x4*)(ab2 + (256 + 512 * wv) * 2);
    const u32x4 a1 = *(const u32x4*)(ab2 + (512 + 512 * wv) * 2);
#define LASTSTEP(W) if (wv == W) { acc[2*W] = mfma16(a0, bh, acc[2*W]); acc[2*W] = mfma16(a0, bl, acc[2*W]); \
                                   acc[2*W+1] = mfma16(a1, bh, acc[2*W+1]); acc[2*W+1] = mfma16(a1, bl, acc[2*W+1]); }
    LASTSTEP(0) LASTSTEP(1) LASTSTEP(2) LASTSTEP(3)
#undef LASTSTEP
  }
  if (acc[0][0] + acc[7][1] == 12345.f) ws[0] = 1;
  STAMPR(3, 3);
  __syncthreads();
  float* red = (float*)lds;
#pragma unroll
  for (int pt = 0; pt < 8; ++pt) *(f32x4*)(red + wv * 2048 + 256 * pt + 16 * c + 4 * kq) = acc[pt];
  __syncthreads();
  {
    f32x4 s0 = *(const f32x4*)(red + tid * 8), s1 = *(const f32x4*)(red + tid * 8 + 4);
#pragma unroll
    for (int w2 = 1; w2 < 4; ++w2) {
      s0 += *(const f32x4*)(red + w2 * 2048 + tid * 8);
      s1 += *(const f32x4*)(red + w2 * 2048 + tid * 8 + 4);
    }
    u16* yT = (u16*)(ws + WS_YT) + (size_t)d * L;
    *(u32x4*)(yT + tid * 8) = pack8(s0, s1);
  }
  STAMPR(3, 4);
}

DI void role_bout(int bid, const u16* __restrict__ wo, u16* __restrict__ out, char* ws, char* lds) {
  const int nt = (bid >> 3) % 7, mt = (bid & 7) * 4 + (bid >> 3) / 7;
  const int tid = threadIdx.x, lane = tid & 63, wv = tid >> 6;
  const int r = lane & 15, kq = lane >> 4;
  const u16* o = (const u16*)(ws + WS_O);
  const int row = mt * 64 + wv * 16 + r;
  u32x4 of[14];
#pragma unroll
  for (int t = 0; t < 14; ++t) of[t] = *(const u32x4*)(o + ((size_t)((mt * 4 + wv) * 14 + t) * 64 + lane) * 8);
  {
    u32x4 v[14];
#pragma unroll
    for (int it = 0; it < 14; ++it) {
      const int idx = tid + 256 * it, rw = idx >> 3, s = idx & 7;
      v[it] = *(const u32x4*)(wo + (size_t)rw * D + nt * 64 + s * 8);
    }
#pragma unroll
    for (int it = 0; it < 14; ++it) {
      const int idx = tid + 256 * it, rw = idx >> 3, s = idx & 7;
      *(u32x4*)(lds + rw * 128 + ((((unsigned)(s >> 1)) ^ wimg_g(rw)) << 5) + ((s & 1) << 4)) = v[it];
    }
  }
  __syncthreads();
  const int q4 = (lane & 15) >> 2, p = lane & 3;
  const unsigned g = (unsigned)((q4 >> 1) | ((kq & 1) << 1));
  f32x4 acc[4];
#pragma unroll
  for (int i = 0; i < 4; ++i) acc[i] = f32x4{0, 0, 0, 0};
  unsigned aoff[4];
#pragma unroll
  for (int ct = 0; ct < 4; ++ct) {
    const int col = 32 * (ct >> 1) + 8 * p + 4 * (ct & 1);
    const unsigned chunk = (unsigned)(col >> 4);
    aoff[ct] = (unsigned)((8 * kq + q4) * 128) + ((chunk ^ g) << 5) + (unsigned)((col & 15) * 2);
  }
#pragma unroll
  for (int t = 0; t < 14; ++t) {
#pragma unroll
    for (int ct = 0; ct < 4; ++ct) {
      const u32x2 lo = tr_read(lds, aoff[ct] + t * 4096u), hi = tr_read(lds, aoff[ct] + t * 4096u + 512u);
      acc[ct] = mfma16(u32x4{lo[0], lo[1], hi[0], hi[1]}, of[t], acc[ct]);
    }
  }
  u16* orow = out + (size_t)row * OUTW + D + nt * 64 + 8 * kq;
  *(u32x4*)(orow) = pack8(acc[0], acc[1]);
  *(u32x4*)(orow + 32) = pack8(acc[2], acc[3]);
}

DI void role_aout(int tile, const u16* __restrict__ u, const u16* __restrict__ dskip, u16* __restrict__ out, char* ws, char* lds) {
  const int lt = tile / 7, dtile = tile % 7;
  const int tid = threadIdx.x;
  u16* t = (u16*)lds;
  const u16* yT = (const u16*)(ws + WS_YT);
  u32x4 uv[2];
#pragma unroll
  for (int i = 0; i < 2; ++i) {
    const int row = (tid >> 3) + 32 * i, ch = tid & 7;
    uv[i] = *(const u32x4*)(u + (size_t)(lt * 64 + row) * D + dtile * 64 + ch * 8);
  }
  const u32x4 dk = *(const u32x4*)(dskip + dtile * 64 + (tid & 7) * 8);
#pragma unroll
  for (int i = 0; i < 2; ++i) {
    const int drow = (tid >> 3) + 32 * i, ch = tid & 7;
    const u32x4 v = *(const u32x4*)(yT + (size_t)(dtile * 64 + drow) * L + lt * 64 + ch * 8);
#pragma unroll
    for (int j = 0; j < 4; ++j) {
      t[(ch * 8 + 2 * j) * 72 + drow] = (u16)(v[j] & 0xffffu);
      t[(ch * 8 + 2 * j + 1) * 72 + drow] = (u16)(v[j] >> 16);
    }
  }
  __syncthreads();
#pragma unroll
  for (int i = 0; i < 2; ++i) {
    const int row = (tid >> 3) + 32 * i, ch = tid & 7;
    const u32x4 y = *(const u32x4*)(t + row * 72 + ch * 8);
    u32x4 o;
#pragma unroll
    for (int j = 0; j < 4; ++j) {
      const float y0 = __uint_as_float(y[j] << 16), y1 = __uint_as_float(y[j] & 0xffff0000u);
      const float u0 = __uint_as_float(uv[i][j] << 16), u1 = __uint_as_float(uv[i][j] & 0xffff0000u);
      const float d0 = __uint_as_float(dk[j] << 16), d1 = __uint_as_float(dk[j] & 0xffff0000u);
      o[j] = pack2(y0 + rbf(u0 * d0), y1 + rbf(u1 * d1));
    }
    *(u32x4*)(out + (size_t)(lt * 64 + row) * OUTW + dtile * 64 + ch * 8) = o;
  }
}

constexpr int LDS1 = 34816;
constexpr int LDS2 = 44608;
constexpr int LDS3 = 448 * 128;

__global__ NOPK __launch_bounds__(256, 3) void k_l1(const u16* u, const u16* logAre, const u16* Aim, const u16* Cre, const u16* Cim,
                                               const u16* logdt, const u16* x, const u16* wqkv, const u16* wphi, char* ws) {
  __shared__ __attribute__((aligned(16))) char lds[LDS1];
  const int b = blockIdx.x;
  if (b < 448) role_kgen(b, logAre, Aim, Cre, Cim, logdt, ws, lds);
  else if (b < 448 + 256) role_b1(b - 448, x, wqkv, wphi, ws, lds);
  else role_utrans(b - 704, u, ws, lds);
}
__global__ NOPK __launch_bounds__(256, 3) void k_l2(const u16* x, const u16* wqkv, const u16* wphi, char* ws) {
  __shared__ __attribute__((aligned(16))) char lds[LDS2];
  const int b = blockIdx.x;
  if (b < 448) role_conv(b, ws, lds);
  else role_bmid(b - 448, wphi, ws, lds);
}
__global__ NOPK __launch_bounds__(256, 2) void k_l3(const u16* u, const u16* dskip, const u16* wo, u16* out, char* ws) {
  __shared__ __attribute__((aligned(16))) char lds[LDS3];
  const int b = blockIdx.x;
  if (b < 224) role_bout(b, wo, out, ws, lds);
  else role_aout(b - 224, u, dskip, out, ws, lds);
}

extern "C" void kernel_launch(void* const* d_in, const int* in_sizes, int n_in, void* d_out, int out_size, void* d_ws,
                              size_t ws_size, hipStream_t stream) {
  const u16* u = (const u16*)d_in[0];
  const u16* logAre = (const u16*)d_in[1];
  const u16* Aim = (const u16*)d_in[2];
  const u16* Cre = (const u16*)d_in[3];
  const u16* Cim = (const u16*)d_in[4];
  const u16* logdt = (const u16*)d_in[5];
  const u16* dskip = (const u16*)d_in[6];
  const u16* x = (const u16*)d_in[7];
  const u16* wqkv = (const u16*)d_in[8];
  const u16* wphi = (const u16*)d_in[9];
  const u16* wo = (const u16*)d_in[10];
  u16* out = (u16*)d_out;
  char* ws = (char*)d_ws;
  k_l1<<<448 + 256 + 224, 256, 0, stream>>>(u, logAre, Aim, Cre, Cim, logdt, x, wqkv, wphi, ws);
  k_l2<<<448 + 128, 256, 0, stream>>>(x, wqkv, wphi, ws);
  k_l3<<<224 + 224, 256, 0, stream>>>(u, dskip, wo, out, ws);
}
```

```cpp
#include <hip/hip_runtime.h>
#include <hip/hip_bf16.h>
#include <stdint.h>

typedef unsigned short u16;
typedef __bf16 bf16x8 __attribute__((ext_vector_type(8)));
typedef float f32x4 __attribute__((ext_vector_type(4)));
typedef short s16x4 __attribute__((ext_vector_type(4)));
typedef unsigned u32x4 __attribute__((ext_vector_type(4)));
typedef unsigned u32x2 __attribute__((ext_vector_type(2)));
#define LDS_AS __attribute__((address_space(3)))
#define DI __device__ __forceinline__
#if defined(__HIP_DEVICE_COMPILE__) && !defined(ALLOW_PK)
#define NOPK __attribute__((target("no-packed-fp32-ops")))
#else
#define NOPK
#endif
#define CFENCE() asm volatile("" ::: "memory")

constexpr int L = 2048, D = 448, NST = 256, H = 16, DH = 28;
constexpr int OUTW = 2 * D;

constexpr int KREV_LEN = 2320;
constexpr int KOFF = 2064;
constexpr size_t WS_UT = 0;
constexpr size_t WS_YT = WS_UT + (size_t)D * L * 2;
constexpr size_t WS_KREV = WS_YT + (size_t)D * L * 2;
constexpr size_t WS_KVP = WS_KREV + (size_t)D * 2 * KREV_LEN * 2;
constexpr size_t WS_O = WS_KVP + (size_t)H * 16 * 2048 * 4;
constexpr size_t WS_QF = WS_O + (size_t)L * D * 2;
constexpr size_t WS_END = WS_QF + (size_t)H * 128 * 1024;

DI float bf2f(u16 v) { return __uint_as_float(((unsigned)v) << 16); }
DI u16 f2bf(float f) { __bf16 b = (__bf16)f; return __builtin_bit_cast(u16, b); }
DI float rbf(float f) { return bf2f(f2bf(f)); }
typedef __bf16 bf16x2 __attribute__((ext_vector_type(2)));
typedef float f32x2 __attribute__((ext_vector_type(2)));
DI unsigned pack2(float a, float b) {
  f32x2 v = {a, b};
  return __builtin_bit_cast(unsigned, __builtin_convertvector(v, bf16x2));
}
DI float fdiv(float n, float d, float rd) { const float q = n * rd; return fmaf(fmaf(-q, d, n), rd, q); }
DI f32x4 mfma16(u32x4 a, u32x4 b, f32x4 c) {
  return __builtin_amdgcn_mfma_f32_16x16x32_bf16(__builtin_bit_cast(bf16x8, a), __builtin_bit_cast(bf16x8, b), c, 0, 0, 0);
}
DI u32x2 tr_read(const char* lds, unsigned off) {
  s16x4 v = __builtin_amdgcn_ds_read_tr16_b64_v4i16((LDS_AS s16x4*)(lds + off));
  return __builtin_bit_cast(u32x2, v);
}

DI unsigned wimg_g(unsigned row) { return ((row >> 1) & 1u) | (((row >> 3) & 1u) << 1); }

DI u32x4 pack8(const f32x4& a, const f32x4& b) {
  u32x4 r = {pack2(a[0], a[1]), pack2(a[2], a[3]), pack2(b[0], b[1]), pack2(b[2], b[3])};
  return r;
}

constexpr int B1_ROWB = 192;
constexpr int B1_XOFF = 64 * B1_ROWB;
struct B1Stage { u32x2 w[8]; u32x4 x[4]; };
DI void b1_load(const u16* __restrict__ wsrc, const u16* __restrict__ xsrc, int c, B1Stage& st) {
  if (threadIdx.x < 192) {
#pragma unroll
    for (int it = 0; it < 8; ++it) st.w[it] = *(const u32x2*)(wsrc + (size_t)(c * 64 + it * 8) * (3 * D));
  }
#pragma unroll
  for (int it = 0; it < 4; ++it) st.x[it] = *(const u32x4*)(xsrc + (size_t)it * 32 * D + c * 64);
}
DI void b1_store(char* wd0, char* wd1, bool wz, char* xd, const B1Stage& st) {
  if (threadIdx.x < 192) {
#pragma unroll
    for (int it = 0; it < 8; ++it) *(u32x2*)(((it & 1) ? wd1 : wd0) + it * 8 * B1_ROWB) = wz ? u32x2{0u, 0u} : st.w[it];
  }
#pragma unroll
  for (int it = 0; it < 4; ++it) *(u32x4*)(xd + it * 32 * 128) = st.x[it];
}
DI u32x4 wphi_frag2(const u16* __restrict__ wphi_h, int lane, int ft) {
  const int f = (lane & 15) + 16 * ft, kq = lane >> 4;
  const int fc = f < DH ? f : DH - 1;
  unsigned e[8];
#pragma unroll
  for (int j = 0; j < 8; ++j) {
    const int d = (j < 4) ? (4 * kq + j) : (16 + 4 * kq + j - 4);
    const unsigned v = wphi_h[(d < DH ? d : DH - 1) * DH + fc];
    e[j] = (d < DH && f < DH) ? v : 0u;
  }
  u32x4 r = {e[0] | (e[1] << 16), e[2] | (e[3] << 16), e[4] | (e[5] << 16), e[6] | (e[7] << 16)};
  return r;
}

DI float xmax(float x) {
  auto r = __builtin_amdgcn_permlane16_swap(__float_as_uint(x), __float_as_uint(x), false, false);
  x = fmaxf(__uint_as_float(r[0]), __uint_as_float(r[1]));
  auto r2 = __builtin_amdgcn_permlane32_swap(__float_as_uint(x), __float_as_uint(x), false, false);
  return fmaxf(__uint_as_float(r2[0]), __uint_as_float(r2[1]));
}
DI float xsum(float x) {
  auto r = __builtin_amdgcn_permlane16_swap(__float_as_uint(x), __float_as_uint(x), false, false);
  x = __uint_as_float(r[0]) + __uint_as_float(r[1]);
  auto r2 = __builtin_amdgcn_permlane32_swap(__float_as_uint(x), __float_as_uint(x), false, false);
  return __uint_as_float(r2[0]) + __uint_as_float(r2[1]);
}
DI void phi_rows(const u32x4& wp0, const u32x4& wp1, const u32x4& xb, bool valid1, u32x4& bp, u32x4& bn) {
  const f32x4 p0 = mfma16(wp0, xb, f32x4{0, 0, 0, 0});
  const f32x4 p1 = mfma16(wp1, xb, f32x4{0, 0, 0, 0});
  float a[4], b[4];
#pragma unroll
  for (int g = 0; g < 4; ++g) { a[g] = rbf(p0[g]); b[g] = rbf(p1[g]); b[g] = valid1 ? b[g] : a[g]; }
  float mx = fmaxf(fmaxf(fmaxf(a[0], a[1]), fmaxf(a[2], a[3])), fmaxf(fmaxf(b[0], b[1]), fmaxf(b[2], b[3])));
  float mn = fminf(fminf(fminf(a[0], a[1]), fminf(a[2], a[3])), fminf(fminf(b[0], b[1]), fminf(b[2], b[3])));
  mx = xmax(mx); mn = -xmax(-mn);
  float ea[4], eb[4], na[4], nb[4];
#pragma unroll
  for (int g = 0; g < 4; ++g) {
    ea[g] = __expf(a[g] - mx); eb[g] = valid1 ? __expf(b[g] - mx) : 0.f;
    na[g] = __expf(mn - a[g]); nb[g] = valid1 ? __expf(mn - b[g]) : 0.f;
  }
  const float sp = xsum(((ea[0] + ea[1]) + (ea[2] + ea[3])) + ((eb[0] + eb[1]) + (eb[2] + eb[3])));
  const float sn = xsum(((na[0] + na[1]) + (na[2] + na[3])) + ((nb[0] + nb[1]) + (nb[2] + nb[3])));
  const float ip = __builtin_amdgcn_rcpf(sp), in = __builtin_amdgcn_rcpf(sn);
  bp = u32x4{pack2(ea[0] * ip, ea[1] * ip), pack2(ea[2] * ip, ea[3] * ip), pack2(eb[0] * ip, eb[1] * ip), pack2(eb[2] * ip, eb[3] * ip)};
  bn = u32x4{pack2(na[0] * in, na[1] * in), pack2(na[2] * in, na[3] * in), pack2(nb[0] * in, nb[1] * in), pack2(nb[2] * in, nb[3] * in)};
}

DI void role_b1(int bid, const u16* __restrict__ x, const u16* __restrict__ wqkv, const u16* __restrict__ wphi,
                char* ws, char* lds) {
  const int h = bid >> 4, chunk = ((bid & 7) << 1) | ((bid >> 3) & 1);
  const int tid = threadIdx.x, lane = tid & 63, wv = tid >> 6;
  const int r = lane & 15, kq = lane >> 4;
  const int row0 = chunk * 128 + wv * 32;
  const int srow = tid / 24, ss = tid % 24;
  const u16* wsrc = wqkv + (size_t)srow * (3 * D) + (ss >> 3) * D + DH * h + 4 * ((ss & 7) < 7 ? (ss & 7) : 6);
  char* wd0 = lds + srow * B1_ROWB + ((ss >> 2) << 5) + ((ss & 3) << 3);
  char* wd1 = lds + srow * B1_ROWB + (((ss >> 2) ^ 1) << 5) + ((ss & 3) << 3);
  const bool wz = (ss & 7) == 7;
  const u16* xsrc = x + (size_t)(chunk * 128 + (tid >> 3)) * D + (tid & 7) * 8;
  char* xd = lds + B1_XOFF + (tid >> 3) * 128 + (((tid & 7) ^ ((tid >> 3) & 7)) << 4);
  B1Stage st;
  b1_load(wsrc, xsrc, 0, st);
  b1_store(wd0, wd1, wz, xd, st);
  b1_load(wsrc, xsrc, 1, st);
  const u16* wphi_h = wphi + h * DH * DH;
  const u32x4 wp0 = wphi_frag2(wphi_h, lane, 0), wp1 = wphi_frag2(wphi_h, lane, 1);
  __syncthreads();
  const int q4 = (lane & 15) >> 2, p = lane & 3;
  const unsigned wbase = (unsigned)((8 * kq + q4) * B1_ROWB + p * 8);
  const unsigned gsw = (unsigned)(kq & 1);
  const char* xr0 = lds + B1_XOFF + (wv * 32 + r) * 128 + ((kq ^ (r & 7)) << 4);
  const char* xr1 = lds + B1_XOFF + (wv * 32 + r) * 128 + (((4 + kq) ^ (r & 7)) << 4);
  f32x4 qT[2][2], kT[2][2], vv[2][2];
#pragma unroll
  for (int i = 0; i < 2; ++i)
#pragma unroll
    for (int j = 0; j < 2; ++j) { qT[i][j] = f32x4{0, 0, 0, 0}; kT[i][j] = f32x4{0, 0, 0, 0}; vv[i][j] = f32x4{0, 0, 0, 0}; }
  auto compute = [&]() {
#pragma unroll
    for (int tt = 0; tt < 2; ++tt) {
      u32x4 xf[2];
#pragma unroll
      for (int rt = 0; rt < 2; ++rt) xf[rt] = *(const u32x4*)((tt ? xr1 : xr0) + rt * 16 * 128);
#pragma unroll
      for (int ct = 0; ct < 2; ++ct) {
        u32x4 wf[3];
#pragma unroll
        for (int pt = 0; pt < 3; ++pt) {
          const unsigned o = wbase + (((unsigned)(2 * pt + ct) ^ gsw) << 5) + (unsigned)tt * (32u * B1_ROWB);
          const u32x2 lo = tr_read(lds, o), hi = tr_read(lds, o + 4u * B1_ROWB);
          wf[pt] = u32x4{lo[0], lo[1], hi[0], hi[1]};
        }
#pragma unroll
        for (int rt = 0; rt < 2; ++rt) {
          qT[ct][rt] = mfma16(wf[0], xf[rt], qT[ct][rt]);
          kT[ct][rt] = mfma16(wf[1], xf[rt], kT[ct][rt]);
          vv[rt][ct] = mfma16(xf[rt], wf[2], vv[rt][ct]);
        }
      }
    }
  };
#pragma unroll 1
  for (int c = 0; c < 6; ++c) {
    compute();
    __syncthreads();
    b1_store(wd0, wd1, wz, xd, st);
    if (c + 2 < 7) b1_load(wsrc, xsrc, c + 2, st);
    __syncthreads();
  }
  compute();
  {
    u32x4* qf = (u32x4*)(ws + WS_QF) + (size_t)(h * 128 + (row0 >> 4)) * 64 + lane;
    qf[0] = pack8(qT[0][0], qT[1][0]);
    qf[64] = pack8(qT[0][1], qT[1][1]);
  }
  f32x4 tp[2][2], tn[2][2];
  {
    const unsigned one = (kq == (r >> 2)) ? ((r & 1) ? 0x3f800000u : 0x00003f80u) : 0u;
    const u32x4 id0 = {(r & 2) ? 0u : one, (r & 2) ? one : 0u, 0u, 0u};
    const u32x4 id1 = {0u, 0u, (r & 2) ? 0u : one, (r & 2) ? one : 0u};
#pragma unroll
    for (int rt = 0; rt < 2; ++rt) {
      u32x4 bp, bn;
      phi_rows(wp0, wp1, pack8(kT[0][rt], kT[1][rt]), kq < 3, bp, bn);
      tp[rt][0] = mfma16(bp, id0, f32x4{0, 0, 0, 0}); tp[rt][1] = mfma16(bp, id1, f32x4{0, 0, 0, 0});
      tn[rt][0] = mfma16(bn, id0, f32x4{0, 0, 0, 0}); tn[rt][1] = mfma16(bn, id1, f32x4{0, 0, 0, 0});
    }
  }
  u32x4 vb[2];
#pragma unroll
  for (int dt = 0; dt < 2; ++dt) vb[dt] = pack8(vv[0][dt], vv[1][dt]);
  if (r == DH - 16) vb[1] = u32x4{0x3f803f80u, 0x3f803f80u, 0x3f803f80u, 0x3f803f80u};
  f32x4 kv[4][2];
#pragma unroll
  for (int ft = 0; ft < 2; ++ft) {
    const u32x4 ap = pack8(tp[0][ft], tp[1][ft]);
    const u32x4 an = pack8(tn[0][ft], tn[1][ft]);
#pragma unroll
    for (int dt = 0; dt < 2; ++dt) {
      kv[ft][dt] = mfma16(ap, vb[dt], f32x4{0, 0, 0, 0});
      kv[2 + ft][dt] = mfma16(an, vb[dt], f32x4{0, 0, 0, 0});
    }
  }
  __syncthreads();
  float* red = (float*)lds;
#pragma unroll
  for (int f4 = 0; f4 < 4; ++f4)
#pragma unroll
    for (int dt = 0; dt < 2; ++dt)
#pragma unroll
      for (int g = 0; g < 4; ++g)
        red[wv * 2048 + (16 * f4 + 4 * kq + g) * 32 + 16 * dt + r] = kv[f4][dt][g];
  __syncthreads();
  float* dst = (float*)(ws + WS_KVP) + (size_t)(h * 16 + chunk) * 2048;
#pragma unroll
  for (int i = 0; i < 2; ++i) {
    const int e = (tid + 256 * i) * 4;
    f32x4 s = *(const f32x4*)(red + e);
    s += *(const f32x4*)(red + 2048 + e);
    s += *(const f32x4*)(red + 4096 + e);
    s += *(const f32x4*)(red + 6144 + e);
    *(f32x4*)(dst + e) = s;
  }
}

struct cplx { float re, im; };
DI cplx cmul(cplx a, cplx b) { return cplx{a.re * b.re - a.im * b.im, a.re * b.im + a.im * b.re}; }

constexpr int KG_STRIDE = 544;
constexpr int KG_WAVE = 16 * KG_STRIDE;

DI u32x2 split_entry(float re, float im) {
  const unsigned h = pack2(re, im);
  const float rr = re - __uint_as_float(h << 16), ri = im - __uint_as_float(h & 0xffff0000u);
  u32x2 o = {h, pack2(rr, ri)};
  return o;
}

constexpr int kpos_index(int l) {
  int pos = l;
  if (l >= 256) {
    const int sh = (l >= 1024) ? 3 : (l >= 512 ? 2 : 1);
    const int half = 1 << (sh - 1);
    const int lsb = (l >> sh) & 1;
    pos = ((l + half - 1 + lsb) >> sh) << sh;
  }
  int row = 40;
  const int j = (pos >> 3) & 15;
  if (pos < 256) row = ((pos >> 7) << 3) + (pos & 7);
  else if (pos < 512) row = 16 + (((pos >> 7) & 1) << 2) + ((pos >> 1) & 3);
  else if (pos < 1024) row = 24 + (((pos >> 7) & 3) << 1) + ((pos >> 2) & 1);
  else if (pos < 2048) row = 32 + ((pos >> 7) & 7);
  return row * 16 + j;
}
struct KIdxTab {
  alignas(16) u16 v[KREV_LEN];
  constexpr KIdxTab() : v() {
    for (int i = 0; i < KREV_LEN; ++i) {
      const int l = KOFF - i;
      v[i] = (u16)(4 * ((l >= 0 && l < L) ? kpos_index(l) : 767));
    }
  }
};
__device__ const KIdxTab KIDX = KIdxTab();

DI void role_kgen(int d, const u16* __restrict__ logAre, const u16* __restrict__ Aim, const u16* __restrict__ Cre,
                  const u16* __restrict__ Cim, const u16* __restrict__ logdt, char* ws, char* lds) {
  const int tid = threadIdx.x, lane = tid & 63, wv = tid >> 6;
  const int n = tid;
  const float dt = rbf(expf(bf2f(logdt[d])));
  const float are = -rbf(expf(bf2f(logAre[d * NST + n])));
  const float ar = are * dt;
  const float ai = bf2f(Aim[d * NST + n]) * dt;
  const cplx C2 = {2.f * (bf2f(Cre[d * NST + n]) * dt), 2.f * (bf2f(Cim[d * NST + n]) * dt)};
  const u32x4 kidx = *(const u32x4*)(KIDX.v + (tid + 2) * 8);
  const float L2E = 1.44269502162933349609f, L2E_LO = 1.92596299112661746e-8f;
  const float I2P = 0.15915493667125701904f, I2P_LO = 6.4206383167e-9f;
  const float LN2 = 0.69314718055994530942f;
  const float xh = ar * L2E;
  const float xl = fmaf(ar, L2E, -xh) + ar * L2E_LO;
  const float th = ai * I2P;
  const float tl = fmaf(ai, I2P, -th) + ai * I2P_LO;
  auto cexp = [&](float m) -> cplx {
    const float mag = __builtin_amdgcn_exp2f(xh * m) * fmaf(xl * m, LN2, 1.f);
    const float T = th * m;
    const float rr = (T - __builtin_rintf(T)) + tl * m;
    return cplx{mag * __builtin_amdgcn_cosf(rr), mag * __builtin_amdgcn_sinf(rr)};
  };
  char* wl = lds + wv * KG_WAVE;
  char* wr = wl + lane * 8;
  const int fi = lane & 15, kq = lane >> 4;
  const char* fr_base = wl + fi * KG_STRIDE + kq * 16;
  u32x4 fb[8];
  {
    cplx F[16];
    F[0] = cplx{1.f, 0.f};
    F[1] = cexp(8.f); F[2] = cexp(16.f); F[4] = cexp(32.f); F[8] = cexp(64.f);
    F[3] = cmul(F[2], F[1]); F[5] = cmul(F[4], F[1]); F[6] = cmul(F[4], F[2]); F[7] = cmul(F[4], F[3]);
#pragma unroll
    for (int j = 1; j < 8; ++j) F[8 + j] = cmul(F[8], F[j]);
#pragma unroll
    for (int j = 0; j < 16; ++j) *(u32x2*)(wr + j * KG_STRIDE) = split_entry(F[j].re, F[j].im);
    CFENCE();
#pragma unroll
    for (int tp = 0; tp < 8; ++tp) fb[tp] = *(const u32x4*)(fr_base + tp * 64);
    CFENCE();
  }
  cplx E[8];
  E[0] = cplx{1.f, 0.f};
  E[1] = cexp(1.f); E[2] = cexp(2.f); E[4] = cexp(4.f);
  E[3] = cmul(E[2], E[1]); E[5] = cmul(E[4], E[1]); E[6] = cmul(E[4], E[2]); E[7] = cmul(E[4], E[3]);
  cplx Hh[17];
  {
    const cplx X1 = cexp(128.f), X2 = cexp(256.f), X4 = cexp(512.f), X8 = cexp(1024.f), X16 = cexp(2048.f);
    Hh[0] = C2; Hh[1] = cmul(C2, X1); Hh[2] = cmul(C2, X2); Hh[3] = cmul(Hh[2], X1);
    Hh[4] = cmul(C2, X4); Hh[5] = cmul(Hh[4], X1); Hh[6] = cmul(Hh[4], X2); Hh[7] = cmul(Hh[6], X1);
    Hh[8] = cmul(C2, X8);
    Hh[9] = cmul(Hh[8], X1); Hh[10] = cmul(Hh[8], X2); Hh[11] = cmul(Hh[10], X1);
    Hh[12] = cmul(Hh[8], X4); Hh[13] = cmul(Hh[12], X1); Hh[14] = cmul(Hh[12], X2); Hh[15] = cmul(Hh[14], X1);
    Hh[16] = cmul(C2, X16);
  }
  f32x4 acc[3];
#pragma unroll
  for (int tile = 0; tile < 3; ++tile) {
#pragma unroll
    for (int rr = 0; rr < 16; ++rr) {
      cplx g;
      const int row = tile * 16 + rr;
      if (row < 16) { const int i = row >> 3, e = row & 7; g = e ? cmul(Hh[i], E[e]) : Hh[i]; }
      else if (row < 24) { const int i = (row - 16) >> 2, e = (row - 16) & 3; g = e ? cmul(Hh[2 + i], E[2 * e]) : Hh[2 + i]; }
      else if (row < 32) { const int i = (row - 24) >> 1, e = (row - 24) & 1; g = e ? cmul(Hh[4 + i], E[4]) : Hh[4 + i]; }
      else if (row < 41) g = Hh[8 + row - 32];
      else g = cplx{0.f, 0.f};
      *(u32x2*)(wr + rr * KG_STRIDE) = split_entry(g.re, -g.im);
    }
    CFENCE();
    f32x4 a0 = {0, 0, 0, 0}, a1 = {0, 0, 0, 0};
#pragma unroll
    for (int tp = 0; tp < 8; ++tp) {
      const u32x4 ga = *(const u32x4*)(fr_base + tp * 64);
      a0 = mfma16(ga, fb[tp], a0);
      a1 = mfma16(ga, u32x4{fb[tp][1], fb[tp][0], fb[tp][3], fb[tp][2]}, a1);
    }
    CFENCE();
    acc[tile] = a0 + a1;
  }
  __syncthreads();
  float* red = (float*)lds;
#pragma unroll
  for (int tile = 0; tile < 3; ++tile)
#pragma unroll
    for (int g = 0; g < 4; ++g) red[wv * 768 + (16 * tile + 4 * kq + g) * 16 + fi] = acc[tile][g];
  __syncthreads();
  if (tid < 192) {
    f32x4 s = *(const f32x4*)(red + tid * 4);
    s += *(const f32x4*)(red + 768 + tid * 4);
    s += *(const f32x4*)(red + 1536 + tid * 4);
    s += *(const f32x4*)(red + 2304 + tid * 4);
    *(f32x4*)(red + 3072 + tid * 4) = s;
  }
  __syncthreads();
  const char* Kc = (const char*)(red + 3072);
  u16* khi = (u16*)(ws + WS_KREV) + (size_t)d * 2 * KREV_LEN;
  u16* klo = khi + KREV_LEN;
  {
    unsigned hh[4], ll[4];
#pragma unroll
    for (int p = 0; p < 4; ++p) {
      const float v0 = *(const float*)(Kc + (kidx[p] & 0xffffu)), v1 = *(const float*)(Kc + (kidx[p] >> 16));
      const u32x2 sp = split_entry(v0, v1);
      hh[p] = sp[0]; ll[p] = sp[1];
    }
    *(u32x4*)(khi + (tid + 2) * 8) = u32x4{hh[0], hh[1], hh[2], hh[3]};
    *(u32x4*)(klo + (tid + 2) * 8) = u32x4{ll[0], ll[1], ll[2], ll[3]};
  }
  if (tid < 34) {
    const int g8 = tid < 2 ? tid : 256 + tid;
    u32x4 h = {0, 0, 0, 0}, l = {0, 0, 0, 0};
    if (g8 == 258) { const u32x2 sp = split_entry(*(const float*)Kc, 0.f); h[0] = sp[0]; l[0] = sp[1]; }
    *(u32x4*)(khi + g8 * 8) = h;
    *(u32x4*)(klo + g8 * 8) = l;
  }
}

DI void tile_put(char* lds, int row, int ch, const u32x4& v) {
  *(u32x4*)(lds + row * 128 + ((((unsigned)(ch >> 1)) ^ wimg_g(row)) << 5) + ((ch & 1) << 4)) = v;
}
DI u32x4 tile_get_T(const char* lds, int lane, int oct, int cb) {
  const int q4 = (lane & 15) >> 2, p = lane & 3;
  const int r0 = 8 * oct + q4;
  const unsigned o = (unsigned)(r0 * 128) + ((((unsigned)cb) ^ wimg_g(r0)) << 5) + (unsigned)(p * 8);
  const u32x2 lo = tr_read(lds, o), hi = tr_read(lds, o + 512u);
  return u32x4{lo[0], lo[1], hi[0], hi[1]};
}
DI void role_utrans(int tile, const u16* __restrict__ u, char* ws, char* lds) {
  const int lt = tile / 7, dtile = tile % 7;
  const int tid = threadIdx.x, lane = tid & 63, wv = tid >> 6;
#pragma unroll
  for (int i = 0; i < 2; ++i) {
    const int row = (tid >> 3) + 32 * i, ch = tid & 7;
    tile_put(lds, row, ch, *(const u32x4*)(u + (size_t)(lt * 64 + row) * D + dtile * 64 + ch * 8));
  }
  __syncthreads();
  u16* uT = (u16*)(ws + WS_UT);
#pragma unroll
  for (int i = 0; i < 2; ++i) {
    const int oct = (lane >> 4) + 4 * i;
    *(u32x4*)(uT + (size_t)(dtile * 64 + wv * 16 + (lane & 15)) * L + lt * 64 + oct * 8) = tile_get_T(lds, lane, oct, wv);
  }
}

DI void role_bmid(int bid, const u16* __restrict__ wphi, char* ws, char* lds) {
  const int h = ((bid & 7) << 1) | ((bid >> 3) & 1), chunk = bid >> 4;
  const int tid = threadIdx.x, lane = tid & 63, wv = tid >> 6;
  const int r = lane & 15, kq = lane >> 4;
  const int rt0 = chunk * 8 + wv * 2;
  u32x4 qb[2];
  {
    const u32x4* qf = (const u32x4*)(ws + WS_QF) + (size_t)(h * 128 + rt0) * 64 + lane;
#pragma unroll
    for (int i = 0; i < 2; ++i) qb[i] = qf[64 * i];
  }
  u16* kvb = (u16*)lds;
  {
    const float* src = (const float*)(ws + WS_KVP) + (size_t)h * 16 * 2048 + tid * 8;
    f32x4 a0[16], a1[16];
#pragma unroll
    for (int c = 0; c < 16; ++c) { a0[c] = *(const f32x4*)(src + c * 2048); a1[c] = *(const f32x4*)(src + c * 2048 + 4); }
    f32x4 s0 = a0[0], s1 = a1[0];
#pragma unroll
    for (int c = 1; c < 16; ++c) { s0 += a0[c]; s1 += a1[c]; }
    *(u32x4*)(kvb + tid * 8) = pack8(s0, s1);
  }
  const u16* wphi_h = wphi + h * DH * DH;
  const u32x4 wp0 = wphi_frag2(wphi_h, lane, 0), wp1 = wphi_frag2(wphi_h, lane, 1);
  __syncthreads();
  u32x4 kva[2][2];
#pragma unroll
  for (int ks = 0; ks < 2; ++ks)
#pragma unroll
    for (int dt = 0; dt < 2; ++dt) {
      unsigned e[8];
#pragma unroll
      for (int j = 0; j < 8; ++j) {
        const int f = (j < 4) ? (4 * kq + j) : (16 + 4 * kq + j - 4);
        e[j] = kvb[(32 * ks + f) * 32 + 16 * dt + r];
      }
      kva[ks][dt] = u32x4{e[0] | (e[1] << 16), e[2] | (e[3] << 16), e[4] | (e[5] << 16), e[6] | (e[7] << 16)};
    }
  u16* o = (u16*)(ws + WS_O);
  const bool valid1 = kq < 3;
  u32x2 r0[2], r1[2];
#pragma unroll
  for (int rt = 0; rt < 2; ++rt) {
    u32x4 bp, bn;
    phi_rows(wp0, wp1, qb[rt], valid1, bp, bn);
    f32x4 n0 = mfma16(kva[0][0], bp, f32x4{0, 0, 0, 0});
    n0 = mfma16(kva[1][0], bn, n0);
    f32x4 n1 = mfma16(kva[0][1], bp, f32x4{0, 0, 0, 0});
    n1 = mfma16(kva[1][1], bn, n1);
    float den = __shfl(n1[0], 48 + r);
    den = rbf(rbf(den) + 1e-6f);
    const float rd = __builtin_amdgcn_rcpf(den);
    r0[rt] = u32x2{pack2(fdiv(rbf(n0[0]), den, rd), fdiv(rbf(n0[1]), den, rd)), pack2(fdiv(rbf(n0[2]), den, rd), fdiv(rbf(n0[3]), den, rd))};
    r1[rt] = u32x2{pack2(fdiv(rbf(n1[0]), den, rd), fdiv(rbf(n1[1]), den, rd)), pack2(fdiv(rbf(n1[2]), den, rd), fdiv(rbf(n1[3]), den, rd))};
  }
  {
    const int col = h * DH + 4 * kq;
    u16* p0 = o + ((size_t)(rt0 * 14 + (col >> 5)) * 64 + ((col >> 3) & 3) * 16 + r) * 8 + (col & 4);
#pragma unroll
    for (int rt = 0; rt < 2; ++rt) *(u32x2*)(p0 + rt * 14 * 512) = r0[rt];
  }
  if (kq < 3) {
    const int col = h * DH + 16 + 4 * kq;
    u16* p1 = o + ((size_t)(rt0 * 14 + (col >> 5)) * 64 + ((col >> 3) & 3) * 16 + r) * 8 + (col & 4);
#pragma unroll
    for (int rt = 0; rt < 2; ++rt) *(u32x2*)(p1 + rt * 14 * 512) = r1[rt];
  }
}

constexpr int UC_STRIDE = 4416;
constexpr int CV_KREV = 8 * UC_STRIDE;
DI void role_conv(int d, char* ws, char* lds) {
  const int tid = threadIdx.x, lane = tid & 63, wv = tid >> 6;
  const u16* uT = (const u16*)(ws + WS_UT) + (size_t)d * L;
  {
    const u32x4* src = (const u32x4*)(ws + WS_KREV + (size_t)d * 2 * KREV_LEN * 2);
    u32x4 v0 = src[tid], v1 = src[tid + 256], v2 = {0, 0, 0, 0};
    if (tid < 580 - 512) v2 = src[tid + 512];
    const u32x4 a = *(const u32x4*)(uT + tid * 8);
    u32x4 b = {0, 0, 0, 0};
    if (tid < 255) b = *(const u32x4*)(uT + tid * 8 + 8);
    *(u32x4*)(lds + CV_KREV + tid * 16) = v0;
    *(u32x4*)(lds + CV_KREV + (tid + 256) * 16) = v1;
    if (tid < 580 - 512) *(u32x4*)(lds + CV_KREV + (tid + 512) * 16) = v2;
    unsigned w[8] = {a[0], a[1], a[2], a[3], b[0], b[1], b[2], b[3]};
#pragma unroll
    for (int s = 0; s < 8; ++s) {
      u32x4 o;
#pragma unroll
      for (int j = 0; j < 4; ++j) {
        const int k = (s >> 1) + j;
        o[j] = (s & 1) ? __builtin_amdgcn_alignbyte(w[k + 1 < 8 ? k + 1 : 7], w[k], 2) : w[k];
      }
      *(u32x4*)(lds + s * UC_STRIDE + 64 + tid * 16) = o;
    }
    if (tid == 0) {
      unsigned z[8] = {0u, 0u, 0u, 0u, a[0], a[1], a[2], a[3]};
#pragma unroll
      for (int sft = 0; sft < 8; ++sft) {
        u32x4 o;
#pragma unroll
        for (int j = 0; j < 4; ++j) {
          const int k = (sft >> 1) + j;
          o[j] = (sft & 1) ? __builtin_amdgcn_alignbyte(z[k + 1], z[k], 2) : z[k];
        }
        *(u32x4*)(lds + sft * UC_STRIDE + 48) = o;
      }
    }
    if (tid >= 64 && tid < 64 + 56) {
      const int i = tid - 64, sft = i & 7, ch = i >> 3;
      *(u32x4*)(lds + sft * UC_STRIDE + (ch < 3 ? ch * 16 : 4160 + (ch - 3) * 16)) = u32x4{0, 0, 0, 0};
    }
  }
  __syncthreads();
  const int c = lane & 15, kq = lane >> 4, r = c;
  const char* abase = lds + (r & 7) * UC_STRIDE + (8 * kq + (r & 8) + 32 * wv) * 2;
  const char* bbase = lds + CV_KREV + (8 * kq - 16 * c + 240 + 32 * wv) * 2;
  f32x4 acc[8];
#pragma unroll
  for (int i = 0; i < 8; ++i) acc[i] = f32x4{0, 0, 0, 0};
#pragma unroll
  for (int b = 0; b < 8; ++b) {
#pragma unroll
    for (int hh = 0; hh < 2; ++hh) {
      const u32x4 bh = *(const u32x4*)(bbase + (256 * b + 128 * hh) * 2);
      const u32x4 bl = *(const u32x4*)(bbase + (256 * b + 128 * hh) * 2 + KREV_LEN * 2);
#pragma unroll
      for (int pt = 7; pt >= 7 - b; --pt) {
        const u32x4 a = *(const u32x4*)(abase + (-1792 + 256 * b + 128 * hh + 256 * pt) * 2);
        acc[pt] = mfma16(a, bh, acc[pt]);
        acc[pt] = mfma16(a, bl, acc[pt]);
      }
    }
  }
  {
    const char* ab2 = abase - 64 * wv;
    const char* bb2 = bbase - 64 * wv;
    const u32x4 bh = *(const u32x4*)(bb2 + 2048 * 2);
    const u32x4 bl = *(const u32x4*)(bb2 + 2048 * 2 + KREV_LEN * 2);
    const u32x4 a0 = *(const u32x4*)(ab2 + (256 + 512 * wv) * 2);
    const u32x4 a1 = *(const u32x4*)(ab2 + (512 + 512 * wv) * 2);
#define LASTSTEP(W) if (wv == W) { acc[2*W] = mfma16(a0, bh, acc[2*W]); acc[2*W] = mfma16(a0, bl, acc[2*W]); \
                                   acc[2*W+1] = mfma16(a1, bh, acc[2*W+1]); acc[2*W+1] = mfma16(a1, bl, acc[2*W+1]); }
    LASTSTEP(0) LASTSTEP(1) LASTSTEP(2) LASTSTEP(3)
#undef LASTSTEP
  }
  __syncthreads();
  float* red = (float*)lds;
#pragma unroll
  for (int pt = 0; pt < 8; ++pt) *(f32x4*)(red + wv * 2048 + 256 * pt + 16 * c + 4 * kq) = acc[pt];
  __syncthreads();
  {
    f32x4 s0 = *(const f32x4*)(red + tid * 8), s1 = *(const f32x4*)(red + tid * 8 + 4);
#pragma unroll
    for (int w2 = 1; w2 < 4; ++w2) {
      s0 += *(const f32x4*)(red + w2 * 2048 + tid * 8);
      s1 += *(const f32x4*)(red + w2 * 2048 + tid * 8 + 4);
    }
    u16* yT = (u16*)(ws + WS_YT) + (size_t)d * L;
    *(u32x4*)(yT + tid * 8) = pack8(s0, s1);
  }
}

DI void role_bout(int bid, const u16* __restrict__ wo, u16* __restrict__ out, char* ws, char* lds) {
  const int nt = (bid >> 3) % 7, mt = (bid & 7) * 4 + (bid >> 3) / 7;
  const int tid = threadIdx.x, lane = tid & 63, wv = tid >> 6;
  const int r = lane & 15, kq = lane >> 4;
  const u16* o = (const u16*)(ws + WS_O);
  const int row = mt * 64 + wv * 16 + r;
  u32x4 of[14];
#pragma unroll
  for (int t = 0; t < 14; ++t) of[t] = *(const u32x4*)(o + ((size_t)((mt * 4 + wv) * 14 + t) * 64 + lane) * 8);
  {
    u32x4 v[14];
#pragma unroll
    for (int it = 0; it < 14; ++it) {
      const int idx = tid + 256 * it, rw = idx >> 3, s = idx & 7;
      v[it] = *(const u32x4*)(wo + (size_t)rw * D + nt * 64 + s * 8);
    }
#pragma unroll
    for (int it = 0; it < 14; ++it) {
      const int idx = tid + 256 * it, rw = idx >> 3, s = idx & 7;
      *(u32x4*)(lds + rw * 128 + ((((unsigned)(s >> 1)) ^ wimg_g(rw)) << 5) + ((s & 1) << 4)) = v[it];
    }
  }
  __syncthreads();
  const int q4 = (lane & 15) >> 2, p = lane & 3;
  const unsigned g = (unsigned)((q4 >> 1) | ((kq & 1) << 1));
  f32x4 acc[4];
#pragma unroll
  for (int i = 0; i < 4; ++i) acc[i] = f32x4{0, 0, 0, 0};
  unsigned aoff[4];
#pragma unroll
  for (int ct = 0; ct < 4; ++ct) {
    const int col = 32 * (ct >> 1) + 8 * p + 4 * (ct & 1);
    const unsigned chunk = (unsigned)(col >> 4);
    aoff[ct] = (unsigned)((8 * kq + q4) * 128) + ((chunk ^ g) << 5) + (unsigned)((col & 15) * 2);
  }
#pragma unroll
  for (int t = 0; t < 14; ++t) {
#pragma unroll
    for (int ct = 0; ct < 4; ++ct) {
      const u32x2 lo = tr_read(lds, aoff[ct] + t * 4096u), hi = tr_read(lds, aoff[ct] + t * 4096u + 512u);
      acc[ct] = mfma16(u32x4{lo[0], lo[1], hi[0], hi[1]}, of[t], acc[ct]);
    }
  }
  u16* orow = out + (size_t)row * OUTW + D + nt * 64 + 8 * kq;
  *(u32x4*)(orow) = pack8(acc[0], acc[1]);
  *(u32x4*)(orow + 32) = pack8(acc[2], acc[3]);
}

DI void role_aout(int tile, const u16* __restrict__ u, const u16* __restrict__ dskip, u16* __restrict__ out, char* ws, char* lds) {
  const int lt = tile / 7, dtile = tile % 7;
  const int tid = threadIdx.x, lane = tid & 63, wv = tid >> 6;
  const u16* yT = (const u16*)(ws + WS_YT);
  const int row = lt * 64 + wv * 16 + (lane & 15);
  u32x4 yv[2], uv[2], dk[2];
#pragma unroll
  for (int i = 0; i < 2; ++i) {
    const int drow = (tid >> 3) + 32 * i, ch = tid & 7;
    yv[i] = *(const u32x4*)(yT + (size_t)(dtile * 64 + drow) * L + lt * 64 + ch * 8);
  }
#pragma unroll
  for (int i = 0; i < 2; ++i) {
    const int oct = (lane >> 4) + 4 * i;
    uv[i] = *(const u32x4*)(u + (size_t)row * D + dtile * 64 + oct * 8);
    dk[i] = *(const u32x4*)(dskip + dtile * 64 + oct * 8);
  }
#pragma unroll
  for (int i = 0; i < 2; ++i) tile_put(lds, (tid >> 3) + 32 * i, tid & 7, yv[i]);
  __syncthreads();
#pragma unroll
  for (int i = 0; i < 2; ++i) {
    const int oct = (lane >> 4) + 4 * i;
    const u32x4 y = tile_get_T(lds, lane, oct, wv);
    u32x4 o;
#pragma unroll
    for (int j = 0; j < 4; ++j) {
      const float y0 = __uint_as_float(y[j] << 16), y1 = __uint_as_float(y[j] & 0xffff0000u);
      const float u0 = __uint_as_float(uv[i][j] << 16), u1 = __uint_as_float(uv[i][j] & 0xffff0000u);
      const float d0 = __uint_as_float(dk[i][j] << 16), d1 = __uint_as_float(dk[i][j] & 0xffff0000u);
      o[j] = pack2(y0 + rbf(u0 * d0), y1 + rbf(u1 * d1));
    }
    *(u32x4*)(out + (size_t)row * OUTW + dtile * 64 + oct * 8) = o;
  }
}

constexpr int LDS1 = 34816;
constexpr int LDS2 = 44608;
constexpr int LDS3 = 448 * 128;

__global__ NOPK __launch_bounds__(256, 3) void k_l1(const u16* u, const u16* logAre, const u16* Aim, const u16* Cre, const u16* Cim,
                                               const u16* logdt, const u16* x, const u16* wqkv, const u16* wphi, char* ws) {
  __shared__ __attribute__((aligned(16))) char lds[LDS1];
  const int b = blockIdx.x;
  if (b < 256) { __builtin_amdgcn_s_setprio(3); role_b1(b, x, wqkv, wphi, ws, lds); }
  else if (b < 256 + 448) role_kgen(b - 256, logAre, Aim, Cre, Cim, logdt, ws, lds);
  else role_utrans(b - 704, u, ws, lds);
}
__global__ NOPK __launch_bounds__(256, 3) void k_l2(const u16* x, const u16* wqkv, const u16* wphi, char* ws) {
  __shared__ __attribute__((aligned(16))) char lds[LDS2];
  const int b = blockIdx.x;
  if (b < 448) { __builtin_amdgcn_s_setprio(3); role_conv(b, ws, lds); }
  else role_bmid(b - 448, wphi, ws, lds);
}
__global__ NOPK __launch_bounds__(256, 2) void k_l3(const u16* u, const u16* dskip, const u16* wo, u16* out, char* ws) {
  __shared__ __attribute__((aligned(16))) char lds[LDS3];
  const int b = blockIdx.x;
  if (b < 224) role_bout(b, wo, out, ws, lds);
  else role_aout(b - 224, u, dskip, out, ws, lds);
}

extern "C" void kernel_launch(void* const* d_in, const int* in_sizes, int n_in, void* d_out, int out_size, void* d_ws,
                              size_t ws_size, hipStream_t stream) {
  const u16* u = (const u16*)d_in[0];
  const u16* logAre = (const u16*)d_in[1];
  const u16* Aim = (const u16*)d_in[2];
  const u16* Cre = (const u16*)d_in[3];
  const u16* Cim = (const u16*)d_in[4];
  const u16* logdt = (const u16*)d_in[5];
  const u16* dskip = (const u16*)d_in[6];
  const u16* x = (const u16*)d_in[7];
  const u16* wqkv = (const u16*)d_in[8];
  const u16* wphi = (const u16*)d_in[9];
  const u16* wo = (const u16*)d_in[10];
  u16* out = (u16*)d_out;
  char* ws = (char*)d_ws;
  k_l1<<<448 + 256 + 224, 256, 0, stream>>>(u, logAre, Aim, Cre, Cim, logdt, x, wqkv, wphi, ws);
  k_l2<<<448 + 256, 256, 0, stream>>>(x, wqkv, wphi, ws);
  k_l3<<<224 + 224, 256, 0, stream>>>(u, dskip, wo, out, ws);
}
```

```cpp
#include <hip/hip_runtime.h>
#include <hip/hip_bf16.h>
#include <stdint.h>

typedef unsigned short u16;
typedef __bf16 bf16x8 __attribute__((ext_vector_type(8)));
typedef float f32x4 __attribute__((ext_vector_type(4)));
typedef short s16x4 __attribute__((ext_vector_type(4)));
typedef unsigned u32x4 __attribute__((ext_vector_type(4)));
typedef unsigned u32x2 __attribute__((ext_vector_type(2)));
#define LDS_AS __attribute__((address_space(3)))
#define DI __device__ __forceinline__
#if defined(__HIP_DEVICE_COMPILE__) && !defined(ALLOW_PK)
#define NOPK __attribute__((target("no-packed-fp32-ops")))
#else
#define NOPK
#endif
#define CFENCE() asm volatile("" ::: "memory")

constexpr int L = 2048, D = 448, NST = 256, H = 16, DH = 28;
constexpr int OUTW = 2 * D;

constexpr int KREV_LEN = 2320;
constexpr int KOFF = 2064;
constexpr size_t WS_UT = 0;
constexpr size_t WS_YT = WS_UT + (size_t)D * L * 2;
constexpr size_t WS_KREV = WS_YT + (size_t)D * L * 2;
constexpr size_t WS_KVP = WS_KREV + (size_t)D * 2 * KREV_LEN * 2;
constexpr size_t WS_O = WS_KVP + (size_t)H * 16 * 2048 * 4;
constexpr size_t WS_QF = WS_O + (size_t)L * D * 2;
constexpr size_t WS_END = WS_QF + (size_t)H * 128 * 1024;

DI float bf2f(u16 v) { return __uint_as_float(((unsigned)v) << 16); }
DI u16 f2bf(float f) { __bf16 b = (__bf16)f; return __builtin_bit_cast(u16, b); }
DI float rbf(float f) { return bf2f(f2bf(f)); }
typedef __bf16 bf16x2 __attribute__((ext_vector_type(2)));
typedef float f32x2 __attribute__((ext_vector_type(2)));
DI unsigned pack2(float a, float b) {
  f32x2 v = {a, b};
  return __builtin_bit_cast(unsigned, __builtin_convertvector(v, bf16x2));
}
DI float fdiv(float n, float d, float rd) { const float q = n * rd; return fmaf(fmaf(-q, d, n), rd, q); }
DI f32x4 mfma16(u32x4 a, u32x4 b, f32x4 c) {
  return __builtin_amdgcn_mfma_f32_16x16x32_bf16(__builtin_bit_cast(bf16x8, a), __builtin_bit_cast(bf16x8, b), c, 0, 0, 0);
}
DI u32x2 tr_read(const char* lds, unsigned off) {
  s16x4 v = __builtin_amdgcn_ds_read_tr16_b64_v4i16((LDS_AS s16x4*)(lds + off));
  return __builtin_bit_cast(u32x2, v);
}

DI unsigned wimg_g(unsigned row) { return ((row >> 1) & 1u) | (((row >> 3) & 1u) << 1); }

DI u32x4 pack8(const f32x4& a, const f32x4& b) {
  u32x4 r = {pack2(a[0], a[1]), pack2(a[2], a[3]), pack2(b[0], b[1]), pack2(b[2], b[3])};
  return r;
}

constexpr int B1_ROWB = 192;
constexpr int B1_XOFF = 64 * B1_ROWB;
struct B1Stage { u32x4 w[4]; u32x4 x[4]; };
DI void b1_load(const u16* __restrict__ wsrc, const u16* __restrict__ xsrc, int c, B1Stage& st) {
  if (threadIdx.x < 192) {
#pragma unroll
    for (int it = 0; it < 4; ++it) st.w[it] = *(const u32x4*)(wsrc + (size_t)(c * 64 + it * 16) * (3 * D));
  }
#pragma unroll
  for (int it = 0; it < 4; ++it) st.x[it] = *(const u32x4*)(xsrc + (size_t)it * 32 * D + c * 64);
}
DI void b1_store(char* wd, bool wl, char* xd, const B1Stage& st) {
  if (threadIdx.x < 192) {
#pragma unroll
    for (int it = 0; it < 4; ++it)
      *(u32x4*)(wd + it * 16 * B1_ROWB) = wl ? u32x4{st.w[it][2], st.w[it][3], 0u, 0u} : st.w[it];
  }
#pragma unroll
  for (int it = 0; it < 4; ++it) *(u32x4*)(xd + it * 32 * 128) = st.x[it];
}
DI u32x4 wphi_frag2(const u16* __restrict__ wphi_h, int lane, int ft) {
  const int f = (lane & 15) + 16 * ft, kq = lane >> 4;
  const int fc = f < DH ? f : DH - 1;
  unsigned e[8];
#pragma unroll
  for (int j = 0; j < 8; ++j) {
    const int d = (j < 4) ? (4 * kq + j) : (16 + 4 * kq + j - 4);
    const unsigned v = wphi_h[(d < DH ? d : DH - 1) * DH + fc];
    e[j] = (d < DH && f < DH) ? v : 0u;
  }
  u32x4 r = {e[0] | (e[1] << 16), e[2] | (e[3] << 16), e[4] | (e[5] << 16), e[6] | (e[7] << 16)};
  return r;
}

DI float xmax(float x) {
  auto r = __builtin_amdgcn_permlane16_swap(__float_as_uint(x), __float_as_uint(x), false, false);
  x = fmaxf(__uint_as_float(r[0]), __uint_as_float(r[1]));
  auto r2 = __builtin_amdgcn_permlane32_swap(__float_as_uint(x), __float_as_uint(x), false, false);
  return fmaxf(__uint_as_float(r2[0]), __uint_as_float(r2[1]));
}
DI float xsum(float x) {
  auto r = __builtin_amdgcn_permlane16_swap(__float_as_uint(x), __float_as_uint(x), false, false);
  x = __uint_as_float(r[0]) + __uint_as_float(r[1]);
  auto r2 = __builtin_amdgcn_permlane32_swap(__float_as_uint(x), __float_as_uint(x), false, false);
  return __uint_as_float(r2[0]) + __uint_as_float(r2[1]);
}
DI void phi_rows(const u32x4& wp0, const u32x4& wp1, const u32x4& xb, bool valid1, u32x4& bp, u32x4& bn) {
  const f32x4 p0 = mfma16(wp0, xb, f32x4{0, 0, 0, 0});
  const f32x4 p1 = mfma16(wp1, xb, f32x4{0, 0, 0, 0});
  float a[4], b[4];
#pragma unroll
  for (int g = 0; g < 4; ++g) { a[g] = rbf(p0[g]); b[g] = rbf(p1[g]); b[g] = valid1 ? b[g] : a[g]; }
  float mx = fmaxf(fmaxf(fmaxf(a[0], a[1]), fmaxf(a[2], a[3])), fmaxf(fmaxf(b[0], b[1]), fmaxf(b[2], b[3])));
  float mn = fminf(fminf(fminf(a[0], a[1]), fminf(a[2], a[3])), fminf(fminf(b[0], b[1]), fminf(b[2], b[3])));
  mx = xmax(mx); mn = -xmax(-mn);
  float ea[4], eb[4], na[4], nb[4];
#pragma unroll
  for (int g = 0; g < 4; ++g) {
    ea[g] = __expf(a[g] - mx); eb[g] = valid1 ? __expf(b[g] - mx) : 0.f;
    na[g] = __expf(mn - a[g]); nb[g] = valid1 ? __expf(mn - b[g]) : 0.f;
  }
  const float sp = xsum(((ea[0] + ea[1]) + (ea[2] + ea[3])) + ((eb[0] + eb[1]) + (eb[2] + eb[3])));
  const float sn = xsum(((na[0] + na[1]) + (na[2] + na[3])) + ((nb[0] + nb[1]) + (nb[2] + nb[3])));
  const float ip = __builtin_amdgcn_rcpf(sp), in = __builtin_amdgcn_rcpf(sn);
  bp = u32x4{pack2(ea[0] * ip, ea[1] * ip), pack2(ea[2] * ip, ea[3] * ip), pack2(eb[0] * ip, eb[1] * ip), pack2(eb[2] * ip, eb[3] * ip)};
  bn = u32x4{pack2(na[0] * in, na[1] * in), pack2(na[2] * in, na[3] * in), pack2(nb[0] * in, nb[1] * in), pack2(nb[2] * in, nb[3] * in)};
}

DI void role_b1(int bid, const u16* __restrict__ x, const u16* __restrict__ wqkv, const u16* __restrict__ wphi,
                char* ws, char* lds) {
  const int h = bid >> 4, chunk = ((bid & 7) << 1) | ((bid >> 3) & 1);
  const int tid = threadIdx.x, lane = tid & 63, wv = tid >> 6;
  const int r = lane & 15, kq = lane >> 4;
  const int row0 = chunk * 128 + wv * 32;
  const int srow = tid / 12, spart = (tid % 12) >> 2, sq = tid & 3;
  const u16* wsrc = wqkv + (size_t)srow * (3 * D) + spart * D + DH * h + (sq < 3 ? 8 * sq : 20);
  char* wd = lds + srow * B1_ROWB + (((2 * spart + (sq >> 1)) ^ ((srow >> 3) & 1)) << 5) + ((sq & 1) << 4);
  const bool wl = sq == 3;
  const u16* xsrc = x + (size_t)(chunk * 128 + (tid >> 3)) * D + (tid & 7) * 8;
  char* xd = lds + B1_XOFF + (tid >> 3) * 128 + (((tid & 7) ^ ((tid >> 3) & 7)) << 4);
  B1Stage st;
  b1_load(wsrc, xsrc, 0, st);
  b1_store(wd, wl, xd, st);
  b1_load(wsrc, xsrc, 1, st);
  const u16* wphi_h = wphi + h * DH * DH;
  const u32x4 wp0 = wphi_frag2(wphi_h, lane, 0), wp1 = wphi_frag2(wphi_h, lane, 1);
  __syncthreads();
  const int q4 = (lane & 15) >> 2, p = lane & 3;
  const unsigned wbase = (unsigned)((8 * kq + q4) * B1_ROWB + p * 8);
  const unsigned gsw = (unsigned)(kq & 1);
  const char* xr0 = lds + B1_XOFF + (wv * 32 + r) * 128 + ((kq ^ (r & 7)) << 4);
  const char* xr1 = lds + B1_XOFF + (wv * 32 + r) * 128 + (((4 + kq) ^ (r & 7)) << 4);
  f32x4 qT[2][2], kT[2][2], vv[2][2];
#pragma unroll
  for (int i = 0; i < 2; ++i)
#pragma unroll
    for (int j = 0; j < 2; ++j) { qT[i][j] = f32x4{0, 0, 0, 0}; kT[i][j] = f32x4{0, 0, 0, 0}; vv[i][j] = f32x4{0, 0, 0, 0}; }
  auto compute = [&]() {
#pragma unroll
    for (int tt = 0; tt < 2; ++tt) {
      u32x4 xf[2];
#pragma unroll
      for (int rt = 0; rt < 2; ++rt) xf[rt] = *(const u32x4*)((tt ? xr1 : xr0) + rt * 16 * 128);
#pragma unroll
      for (int ct = 0; ct < 2; ++ct) {
        u32x4 wf[3];
#pragma unroll
        for (int pt = 0; pt < 3; ++pt) {
          const unsigned o = wbase + (((unsigned)(2 * pt + ct) ^ gsw) << 5) + (unsigned)tt * (32u * B1_ROWB);
          const u32x2 lo = tr_read(lds, o), hi = tr_read(lds, o + 4u * B1_ROWB);
          wf[pt] = u32x4{lo[0], lo[1], hi[0], hi[1]};
        }
#pragma unroll
        for (int rt = 0; rt < 2; ++rt) {
          qT[ct][rt] = mfma16(wf[0], xf[rt], qT[ct][rt]);
          kT[ct][rt] = mfma16(wf[1], xf[rt], kT[ct][rt]);
          vv[rt][ct] = mfma16(xf[rt], wf[2], vv[rt][ct]);
        }
      }
    }
  };
#pragma unroll 1
  for (int c = 0; c < 6; ++c) {
    compute();
    __syncthreads();
    b1_store(wd, wl, xd, st);
    if (c + 2 < 7) b1_load(wsrc, xsrc, c + 2, st);
    __syncthreads();
  }
  compute();
  {
    u32x4* qf = (u32x4*)(ws + WS_QF) + (size_t)(h * 128 + (row0 >> 4)) * 64 + lane;
    qf[0] = pack8(qT[0][0], qT[1][0]);
    qf[64] = pack8(qT[0][1], qT[1][1]);
  }
  f32x4 tp[2][2], tn[2][2];
  {
    const unsigned one = (kq == (r >> 2)) ? ((r & 1) ? 0x3f800000u : 0x00003f80u) : 0u;
    const u32x4 id0 = {(r & 2) ? 0u : one, (r & 2) ? one : 0u, 0u, 0u};
    const u32x4 id1 = {0u, 0u, (r & 2) ? 0u : one, (r & 2) ? one : 0u};
#pragma unroll
    for (int rt = 0; rt < 2; ++rt) {
      u32x4 bp, bn;
      phi_rows(wp0, wp1, pack8(kT[0][rt], kT[1][rt]), kq < 3, bp, bn);
      tp[rt][0] = mfma16(bp, id0, f32x4{0, 0, 0, 0}); tp[rt][1] = mfma16(bp, id1, f32x4{0, 0, 0, 0});
      tn[rt][0] = mfma16(bn, id0, f32x4{0, 0, 0, 0}); tn[rt][1] = mfma16(bn, id1, f32x4{0, 0, 0, 0});
    }
  }
  u32x4 vb[2];
#pragma unroll
  for (int dt = 0; dt < 2; ++dt) vb[dt] = pack8(vv[0][dt], vv[1][dt]);
  if (r == DH - 16) vb[1] = u32x4{0x3f803f80u, 0x3f803f80u, 0x3f803f80u, 0x3f803f80u};
  f32x4 kv[4][2];
#pragma unroll
  for (int ft = 0; ft < 2; ++ft) {
    const u32x4 ap = pack8(tp[0][ft], tp[1][ft]);
    const u32x4 an = pack8(tn[0][ft], tn[1][ft]);
#pragma unroll
    for (int dt = 0; dt < 2; ++dt) {
      kv[ft][dt] = mfma16(ap, vb[dt], f32x4{0, 0, 0, 0});
      kv[2 + ft][dt] = mfma16(an, vb[dt], f32x4{0, 0, 0, 0});
    }
  }
  __syncthreads();
  float* red = (float*)lds;
#pragma unroll
  for (int f4 = 0; f4 < 4; ++f4)
#pragma unroll
    for (int dt = 0; dt < 2; ++dt)
#pragma unroll
      for (int g = 0; g < 4; ++g)
        red[wv * 2048 + (16 * f4 + 4 * kq + g) * 32 + 16 * dt + r] = kv[f4][dt][g];
  __syncthreads();
  float* dst = (float*)(ws + WS_KVP) + (size_t)(h * 16 + chunk) * 2048;
#pragma unroll
  for (int i = 0; i < 2; ++i) {
    const int e = (tid + 256 * i) * 4;
    f32x4 s = *(const f32x4*)(red + e);
    s += *(const f32x4*)(red + 2048 + e);
    s += *(const f32x4*)(red + 4096 + e);
    s += *(const f32x4*)(red + 6144 + e);
    *(f32x4*)(dst + e) = s;
  }
}

struct cplx { float re, im; };
DI cplx cmul(cplx a, cplx b) { return cplx{a.re * b.re - a.im * b.im, a.re * b.im + a.im * b.re}; }

constexpr int KG_STRIDE = 544;
constexpr int KG_WAVE = 16 * KG_STRIDE;

DI u32x2 split_entry(float re, float im) {
  const unsigned h = pack2(re, im);
  const float rr = re - __uint_as_float(h << 16), ri = im - __uint_as_float(h & 0xffff0000u);
  u32x2 o = {h, pack2(rr, ri)};
  return o;
}

constexpr int kpos_index(int l) {
  int pos = l;
  if (l >= 256) {
    const int sh = (l >= 1024) ? 3 : (l >= 512 ? 2 : 1);
    const int half = 1 << (sh - 1);
    const int lsb = (l >> sh) & 1;
    pos = ((l + half - 1 + lsb) >> sh) << sh;
  }
  int row = 40;
  const int j = (pos >> 3) & 15;
  if (pos < 256) row = ((pos >> 7) << 3) + (pos & 7);
  else if (pos < 512) row = 16 + (((pos >> 7) & 1) << 2) + ((pos >> 1) & 3);
  else if (pos < 1024) row = 24 + (((pos >> 7) & 3) << 1) + ((pos >> 2) & 1);
  else if (pos < 2048) row = 32 + ((pos >> 7) & 7);
  return row * 16 + j;
}
struct KIdxTab {
  alignas(16) u16 v[KREV_LEN];
  constexpr KIdxTab() : v() {
    for (int i = 0; i < KREV_LEN; ++i) {
      const int l = KOFF - i;
      v[i] = (u16)(4 * ((l >= 0 && l < L) ? kpos_index(l) : 767));
    }
  }
};
__device__ const KIdxTab KIDX = KIdxTab();

DI void role_kgen(int d, const u16* __restrict__ logAre, const u16* __restrict__ Aim, const u16* __restrict__ Cre,
                  const u16* __restrict__ Cim, const u16* __restrict__ logdt, char* ws, char* lds) {
  const int tid = threadIdx.x, lane = tid & 63, wv = tid >> 6;
  const int n = tid;
  const float dt = rbf(expf(bf2f(logdt[d])));
  const float are = -rbf(expf(bf2f(logAre[d * NST + n])));
  const float ar = are * dt;
  const float ai = bf2f(Aim[d * NST + n]) * dt;
  const cplx C2 = {2.f * (bf2f(Cre[d * NST + n]) * dt), 2.f * (bf2f(Cim[d * NST + n]) * dt)};
  const u32x4 kidx = *(const u32x4*)(KIDX.v + (tid + 2) * 8);
  const float L2E = 1.44269502162933349609f, L2E_LO = 1.92596299112661746e-8f;
  const float I2P = 0.15915493667125701904f, I2P_LO = 6.4206383167e-9f;
  const float LN2 = 0.69314718055994530942f;
  const float xh = ar * L2E;
  const float xl = fmaf(ar, L2E, -xh) + ar * L2E_LO;
  const float th = ai * I2P;
  const float tl = fmaf(ai, I2P, -th) + ai * I2P_LO;
  auto cexp = [&](float m) -> cplx {
    const float mag = __builtin_amdgcn_exp2f(xh * m) * fmaf(xl * m, LN2, 1.f);
    const float T = th * m;
    const float rr = (T - __builtin_rintf(T)) + tl * m;
    return cplx{mag * __builtin_amdgcn_cosf(rr), mag * __builtin_amdgcn_sinf(rr)};
  };
  char* wl = lds + wv * KG_WAVE;
  char* wr = wl + lane * 8;
  const int fi = lane & 15, kq = lane >> 4;
  const char* fr_base = wl + fi * KG_STRIDE + kq * 16;
  u32x4 fb[8];
  {
    cplx F[16];
    F[0] = cplx{1.f, 0.f};
    F[1] = cexp(8.f); F[2] = cexp(16.f); F[4] = cexp(32.f); F[8] = cexp(64.f);
    F[3] = cmul(F[2], F[1]); F[5] = cmul(F[4], F[1]); F[6] = cmul(F[4], F[2]); F[7] = cmul(F[4], F[3]);
#pragma unroll
    for (int j = 1; j < 8; ++j) F[8 + j] = cmul(F[8], F[j]);
#pragma unroll
    for (int j = 0; j < 16; ++j) *(u32x2*)(wr + j * KG_STRIDE) = split_entry(F[j].re, F[j].im);
    CFENCE();
#pragma unroll
    for (int tp = 0; tp < 8; ++tp) fb[tp] = *(const u32x4*)(fr_base + tp * 64);
    CFENCE();
  }
  cplx E[8];
  E[0] = cplx{1.f, 0.f};
  E[1] = cexp(1.f); E[2] = cexp(2.f); E[4] = cexp(4.f);
  E[3] = cmul(E[2], E[1]); E[5] = cmul(E[4], E[1]); E[6] = cmul(E[4], E[2]); E[7] = cmul(E[4], E[3]);
  cplx Hh[17];
  {
    const cplx X1 = cexp(128.f), X2 = cexp(256.f), X4 = cexp(512.f), X8 = cexp(1024.f), X16 = cexp(2048.f);
    Hh[0] = C2; Hh[1] = cmul(C2, X1); Hh[2] = cmul(C2, X2); Hh[3] = cmul(Hh[2], X1);
    Hh[4] = cmul(C2, X4); Hh[5] = cmul(Hh[4], X1); Hh[6] = cmul(Hh[4], X2); Hh[7] = cmul(Hh[6], X1);
    Hh[8] = cmul(C2, X8);
    Hh[9] = cmul(Hh[8], X1); Hh[10] = cmul(Hh[8], X2); Hh[11] = cmul(Hh[10], X1);
    Hh[12] = cmul(Hh[8], X4); Hh[13] = cmul(Hh[12], X1); Hh[14] = cmul(Hh[12], X2); Hh[15] = cmul(Hh[14], X1);
    Hh[16] = cmul(C2, X16);
  }
  f32x4 acc[3];
#pragma unroll
  for (int tile = 0; tile < 3; ++tile) {
#pragma unroll
    for (int rr = 0; rr < 16; ++rr) {
      cplx g;
      const int row = tile * 16 + rr;
      if (row < 16) { const int i = row >> 3, e = row & 7; g = e ? cmul(Hh[i], E[e]) : Hh[i]; }
      else if (row < 24) { const int i = (row - 16) >> 2, e = (row - 16) & 3; g = e ? cmul(Hh[2 + i], E[2 * e]) : Hh[2 + i]; }
      else if (row < 32) { const int i = (row - 24) >> 1, e = (row - 24) & 1; g = e ? cmul(Hh[4 + i], E[4]) : Hh[4 + i]; }
      else if (row < 41) g = Hh[8 + row - 32];
      else g = cplx{0.f, 0.f};
      *(u32x2*)(wr + rr * KG_STRIDE) = split_entry(g.re, -g.im);
    }
    CFENCE();
    f32x4 a0 = {0, 0, 0, 0}, a1 = {0, 0, 0, 0};
#pragma unroll
    for (int tp = 0; tp < 8; ++tp) {
      const u32x4 ga = *(const u32x4*)(fr_base + tp * 64);
      a0 = mfma16(ga, fb[tp], a0);
      a1 = mfma16(ga, u32x4{fb[tp][1], fb[tp][0], fb[tp][3], fb[tp][2]}, a1);
    }
    CFENCE();
    acc[tile] = a0 + a1;
  }
  __syncthreads();
  float* red = (float*)lds;
#pragma unroll
  for (int tile = 0; tile < 3; ++tile)
#pragma unroll
    for (int g = 0; g < 4; ++g) red[wv * 768 + (16 * tile + 4 * kq + g) * 16 + fi] = acc[tile][g];
  __syncthreads();
  if (tid < 192) {
    f32x4 s = *(const f32x4*)(red + tid * 4);
    s += *(const f32x4*)(red + 768 + tid * 4);
    s += *(const f32x4*)(red + 1536 + tid * 4);
    s += *(const f32x4*)(red + 2304 + tid * 4);
    *(f32x4*)(red + 3072 + tid * 4) = s;
  }
  __syncthreads();
  const char* Kc = (const char*)(red + 3072);
  u16* khi = (u16*)(ws + WS_KREV) + (size_t)d * 2 * KREV_LEN;
  u16* klo = khi + KREV_LEN;
  {
    unsigned hh[4], ll[4];
#pragma unroll
    for (int p = 0; p < 4; ++p) {
      const float v0 = *(const float*)(Kc + (kidx[p] & 0xffffu)), v1 = *(const float*)(Kc + (kidx[p] >> 16));
      const u32x2 sp = split_entry(v0, v1);
      hh[p] = sp[0]; ll[p] = sp[1];
    }
    *(u32x4*)(khi + (tid + 2) * 8) = u32x4{hh[0], hh[1], hh[2], hh[3]};
    *(u32x4*)(klo + (tid + 2) * 8) = u32x4{ll[0], ll[1], ll[2], ll[3]};
  }
  if (tid < 34) {
    const int g8 = tid < 2 ? tid : 256 + tid;
    u32x4 h = {0, 0, 0, 0}, l = {0, 0, 0, 0};
    if (g8 == 258) { const u32x2 sp = split_entry(*(const float*)Kc, 0.f); h[0] = sp[0]; l[0] = sp[1]; }
    *(u32x4*)(khi + g8 * 8) = h;
    *(u32x4*)(klo + g8 * 8) = l;
  }
}

DI void tile_put(char* lds, int row, int ch, const u32x4& v) {
  *(u32x4*)(lds + row * 128 + ((((unsigned)(ch >> 1)) ^ wimg_g(row)) << 5) + ((ch & 1) << 4)) = v;
}
DI u32x4 tile_get_T(const char* lds, int lane, int oct, int cb) {
  const int q4 = (lane & 15) >> 2, p = lane & 3;
  const int r0 = 8 * oct + q4;
  const unsigned o = (unsigned)(r0 * 128) + ((((unsigned)cb) ^ wimg_g(r0)) << 5) + (unsigned)(p * 8);
  const u32x2 lo = tr_read(lds, o), hi = tr_read(lds, o + 512u);
  return u32x4{lo[0], lo[1], hi[0], hi[1]};
}
DI void role_utrans(int tile, const u16* __restrict__ u, char* ws, char* lds) {
  const int lt = tile / 7, dtile = tile % 7;
  const int tid = threadIdx.x, lane = tid & 63, wv = tid >> 6;
#pragma unroll
  for (int i = 0; i < 2; ++i) {
    const int row = (tid >> 3) + 32 * i, ch = tid & 7;
    tile_put(lds, row, ch, *(const u32x4*)(u + (size_t)(lt * 64 + row) * D + dtile * 64 + ch * 8));
  }
  __syncthreads();
  u16* uT = (u16*)(ws + WS_UT);
#pragma unroll
  for (int i = 0; i < 2; ++i) {
    const int oct = (lane >> 4) + 4 * i;
    *(u32x4*)(uT + (size_t)(dtile * 64 + wv * 16 + (lane & 15)) * L + lt * 64 + oct * 8) = tile_get_T(lds, lane, oct, wv);
  }
}

DI void role_bmid(int bid, const u16* __restrict__ wphi, char* ws, char* lds) {
  const int h = ((bid & 7) << 1) | ((bid >> 3) & 1), chunk = bid >> 4;
  const int tid = threadIdx.x, lane = tid & 63, wv = tid >> 6;
  const int r = lane & 15, kq = lane >> 4;
  const int rt0 = chunk * 8 + wv * 2;
  u32x4 qb[2];
  {
    const u32x4* qf = (const u32x4*)(ws + WS_QF) + (size_t)(h * 128 + rt0) * 64 + lane;
#pragma unroll
    for (int i = 0; i < 2; ++i) qb[i] = qf[64 * i];
  }
  u16* kvb = (u16*)lds;
  {
    const float* src = (const float*)(ws + WS_KVP) + (size_t)h * 16 * 2048 + tid * 8;
    f32x4 a0[16], a1[16];
#pragma unroll
    for (int c = 0; c < 16; ++c) { a0[c] = *(const f32x4*)(src + c * 2048); a1[c] = *(const f32x4*)(src + c * 2048 + 4); }
    f32x4 s0 = a0[0], s1 = a1[0];
#pragma unroll
    for (int c = 1; c < 16; ++c) { s0 += a0[c]; s1 += a1[c]; }
    *(u32x4*)(kvb + tid * 8) = pack8(s0, s1);
  }
  const u16* wphi_h = wphi + h * DH * DH;
  const u32x4 wp0 = wphi_frag2(wphi_h, lane, 0), wp1 = wphi_frag2(wphi_h, lane, 1);
  __syncthreads();
  u32x4 kva[2][2];
#pragma unroll
  for (int ks = 0; ks < 2; ++ks)
#pragma unroll
    for (int dt = 0; dt < 2; ++dt) {
      unsigned e[8];
#pragma unroll
      for (int j = 0; j < 8; ++j) {
        const int f = (j < 4) ? (4 * kq + j) : (16 + 4 * kq + j - 4);
        e[j] = kvb[(32 * ks + f) * 32 + 16 * dt + r];
      }
      kva[ks][dt] = u32x4{e[0] | (e[1] << 16), e[2] | (e[3] << 16), e[4] | (e[5] << 16), e[6] | (e[7] << 16)};
    }
  u16* o = (u16*)(ws + WS_O);
  const bool valid1 = kq < 3;
  u32x2 r0[2], r1[2];
#pragma unroll
  for (int rt = 0; rt < 2; ++rt) {
    u32x4 bp, bn;
    phi_rows(wp0, wp1, qb[rt], valid1, bp, bn);
    f32x4 n0 = mfma16(kva[0][0], bp, f32x4{0, 0, 0, 0});
    n0 = mfma16(kva[1][0], bn, n0);
    f32x4 n1 = mfma16(kva[0][1], bp, f32x4{0, 0, 0, 0});
    n1 = mfma16(kva[1][1], bn, n1);
    float den = __shfl(n1[0], 48 + r);
    den = rbf(rbf(den) + 1e-6f);
    const float rd = __builtin_amdgcn_rcpf(den);
    r0[rt] = u32x2{pack2(fdiv(rbf(n0[0]), den, rd), fdiv(rbf(n0[1]), den, rd)), pack2(fdiv(rbf(n0[2]), den, rd), fdiv(rbf(n0[3]), den, rd))};
    r1[rt] = u32x2{pack2(fdiv(rbf(n1[0]), den, rd), fdiv(rbf(n1[1]), den, rd)), pack2(fdiv(rbf(n1[2]), den, rd), fdiv(rbf(n1[3]), den, rd))};
  }
  {
    const int col = h * DH + 4 * kq;
    u16* p0 = o + ((size_t)(rt0 * 14 + (col >> 5)) * 64 + ((col >> 3) & 3) * 16 + r) * 8 + (col & 4);
#pragma unroll
    for (int rt = 0; rt < 2; ++rt) *(u32x2*)(p0 + rt * 14 * 512) = r0[rt];
  }
  if (kq < 3) {
    const int col = h * DH + 16 + 4 * kq;
    u16* p1 = o + ((size_t)(rt0 * 14 + (col >> 5)) * 64 + ((col >> 3) & 3) * 16 + r) * 8 + (col & 4);
#pragma unroll
    for (int rt = 0; rt < 2; ++rt) *(u32x2*)(p1 + rt * 14 * 512) = r1[rt];
  }
}

constexpr int UC_STRIDE = 4416;
constexpr int CV_KREV = 8 * UC_STRIDE;
DI void role_conv(int d, char* ws, char* lds) {
  const int tid = threadIdx.x, lane = tid & 63, wv = tid >> 6;
  const u16* uT = (const u16*)(ws + WS_UT) + (size_t)d * L;
  {
    const u32x4* src = (const u32x4*)(ws + WS_KREV + (size_t)d * 2 * KREV_LEN * 2);
    u32x4 v0 = src[tid], v1 = src[tid + 256], v2 = {0, 0, 0, 0};
    if (tid < 580 - 512) v2 = src[tid + 512];
    const u32x4 a = *(const u32x4*)(uT + tid * 8);
    u32x4 b = {0, 0, 0, 0};
    if (tid < 255) b = *(const u32x4*)(uT + tid * 8 + 8);
    *(u32x4*)(lds + CV_KREV + tid * 16) = v0;
    *(u32x4*)(lds + CV_KREV + (tid + 256) * 16) = v1;
    if (tid < 580 - 512) *(u32x4*)(lds + CV_KREV + (tid + 512) * 16) = v2;
    unsigned w[8] = {a[0], a[1], a[2], a[3], b[0], b[1], b[2], b[3]};
#pragma unroll
    for (int s = 0; s < 8; ++s) {
      u32x4 o;
#pragma unroll
      for (int j = 0; j < 4; ++j) {
        const int k = (s >> 1) + j;
        o[j] = (s & 1) ? __builtin_amdgcn_alignbyte(w[k + 1 < 8 ? k + 1 : 7], w[k], 2) : w[k];
      }
      *(u32x4*)(lds + s * UC_STRIDE + 64 + tid * 16) = o;
    }
    if (tid == 0) {
      unsigned z[8] = {0u, 0u, 0u, 0u, a[0], a[1], a[2], a[3]};
#pragma unroll
      for (int sft = 0; sft < 8; ++sft) {
        u32x4 o;
#pragma unroll
        for (int j = 0; j < 4; ++j) {
          const int k = (sft >> 1) + j;
          o[j] = (sft & 1) ? __builtin_amdgcn_alignbyte(z[k + 1], z[k], 2) : z[k];
        }
        *(u32x4*)(lds + sft * UC_STRIDE + 48) = o;
      }
    }
    if (tid >= 64 && tid < 64 + 56) {
      const int i = tid - 64, sft = i & 7, ch = i >> 3;
      *(u32x4*)(lds + sft * UC_STRIDE + (ch < 3 ? ch * 16 : 4160 + (ch - 3) * 16)) = u32x4{0, 0, 0, 0};
    }
  }
  __syncthreads();
  const int c = lane & 15, kq = lane >> 4, r = c;
  const char* abase = lds + (r & 7) * UC_STRIDE + (8 * kq + (r & 8) + 32 * wv) * 2;
  const char* bbase = lds + CV_KREV + (8 * kq - 16 * c + 240 + 32 * wv) * 2;
  f32x4 acc[8];
#pragma unroll
  for (int i = 0; i < 8; ++i) acc[i] = f32x4{0, 0, 0, 0};
#pragma unroll
  for (int b = 0; b < 8; ++b) {
#pragma unroll
    for (int hh = 0; hh < 2; ++hh) {
      const u32x4 bh = *(const u32x4*)(bbase + (256 * b + 128 * hh) * 2);
      const u32x4 bl = *(const u32x4*)(bbase + (256 * b + 128 * hh) * 2 + KREV_LEN * 2);
#pragma unroll
      for (int pt = 7; pt >= 7 - b; --pt) {
        const u32x4 a = *(const u32x4*)(abase + (-1792 + 256 * b + 128 * hh + 256 * pt) * 2);
        acc[pt] = mfma16(a, bh, acc[pt]);
        acc[pt] = mfma16(a, bl, acc[pt]);
      }
    }
  }
  {
    const char* ab2 = abase - 64 * wv;
    const char* bb2 = bbase - 64 * wv;
    const u32x4 bh = *(const u32x4*)(bb2 + 2048 * 2);
    const u32x4 bl = *(const u32x4*)(bb2 + 2048 * 2 + KREV_LEN * 2);
    const u32x4 a0 = *(const u32x4*)(ab2 + (256 + 512 * wv) * 2);
    const u32x4 a1 = *(const u32x4*)(ab2 + (512 + 512 * wv) * 2);
#define LASTSTEP(W) if (wv == W) { acc[2*W] = mfma16(a0, bh, acc[2*W]); acc[2*W] = mfma16(a0, bl, acc[2*W]); \
                                   acc[2*W+1] = mfma16(a1, bh, acc[2*W+1]); acc[2*W+1] = mfma16(a1, bl, acc[2*W+1]); }
    LASTSTEP(0) LASTSTEP(1) LASTSTEP(2) LASTSTEP(3)
#undef LASTSTEP
  }
  __syncthreads();
  float* red = (float*)lds;
#pragma unroll
  for (int pt = 0; pt < 8; ++pt) *(f32x4*)(red + wv * 2048 + 256 * pt + 16 * c + 4 * kq) = acc[pt];
  __syncthreads();
  {
    f32x4 s0 = *(const f32x4*)(red + tid * 8), s1 = *(const f32x4*)(red + tid * 8 + 4);
#pragma unroll
    for (int w2 = 1; w2 < 4; ++w2) {
      s0 += *(const f32x4*)(red + w2 * 2048 + tid * 8);
      s1 += *(const f32x4*)(red + w2 * 2048 + tid * 8 + 4);
    }
    u16* yT = (u16*)(ws + WS_YT) + (size_t)d * L;
    *(u32x4*)(yT + tid * 8) = pack8(s0, s1);
  }
}

DI void role_bout(int bid, const u16* __restrict__ wo, u16* __restrict__ out, char* ws, char* lds) {
  const int nt = (bid >> 3) % 7, mt = (bid & 7) * 4 + (bid >> 3) / 7;
  const int tid = threadIdx.x, lane = tid & 63, wv = tid >> 6;
  const int r = lane & 15, kq = lane >> 4;
  const u16* o = (const u16*)(ws + WS_O);
  const int row = mt * 64 + wv * 16 + r;
  u32x4 of[14];
#pragma unroll
  for (int t = 0; t < 14; ++t) of[t] = *(const u32x4*)(o + ((size_t)((mt * 4 + wv) * 14 + t) * 64 + lane) * 8);
  {
    u32x4 v[14];
#pragma unroll
    for (int it = 0; it < 14; ++it) {
      const int idx = tid + 256 * it, rw = idx >> 3, s = idx & 7;
      v[it] = *(const u32x4*)(wo + (size_t)rw * D + nt * 64 + s * 8);
    }
#pragma unroll
    for (int it = 0; it < 14; ++it) {
      const int idx = tid + 256 * it, rw = idx >> 3, s = idx & 7;
      *(u32x4*)(lds + rw * 128 + ((((unsigned)(s >> 1)) ^ wimg_g(rw)) << 5) + ((s & 1) << 4)) = v[it];
    }
  }
  __syncthreads();
  const int q4 = (lane & 15) >> 2, p = lane & 3;
  const unsigned g = (unsigned)((q4 >> 1) | ((kq & 1) << 1));
  f32x4 acc[4];
#pragma unroll
  for (int i = 0; i < 4; ++i) acc[i] = f32x4{0, 0, 0, 0};
  unsigned aoff[4];
#pragma unroll
  for (int ct = 0; ct < 4; ++ct) {
    const int col = 32 * (ct >> 1) + 8 * p + 4 * (ct & 1);
    const unsigned chunk = (unsigned)(col >> 4);
    aoff[ct] = (unsigned)((8 * kq + q4) * 128) + ((chunk ^ g) << 5) + (unsigned)((col & 15) * 2);
  }
#pragma unroll
  for (int t = 0; t < 14; ++t) {
#pragma unroll
    for (int ct = 0; ct < 4; ++ct) {
      const u32x2 lo = tr_read(lds, aoff[ct] + t * 4096u), hi = tr_read(lds, aoff[ct] + t * 4096u + 512u);
      acc[ct] = mfma16(u32x4{lo[0], lo[1], hi[0], hi[1]}, of[t], acc[ct]);
    }
  }
  u16* orow = out + (size_t)row * OUTW + D + nt * 64 + 8 * kq;
  *(u32x4*)(orow) = pack8(acc[0], acc[1]);
  *(u32x4*)(orow + 32) = pack8(acc[2], acc[3]);
}

DI void role_aout(int tile, const u16* __restrict__ u, const u16* __restrict__ dskip, u16* __restrict__ out, char* ws, char* lds) {
  const int lt = tile / 7, dtile = tile % 7;
  const int tid = threadIdx.x, lane = tid & 63, wv = tid >> 6;
  const u16* yT = (const u16*)(ws + WS_YT);
  const int row = lt * 64 + wv * 16 + (lane & 15);
  u32x4 yv[2], uv[2], dk[2];
#pragma unroll
  for (int i = 0; i < 2; ++i) {
    const int drow = (tid >> 3) + 32 * i, ch = tid & 7;
    yv[i] = *(const u32x4*)(yT + (size_t)(dtile * 64 + drow) * L + lt * 64 + ch * 8);
  }
#pragma unroll
  for (int i = 0; i < 2; ++i) {
    const int oct = (lane >> 4) + 4 * i;
    uv[i] = *(const u32x4*)(u + (size_t)row * D + dtile * 64 + oct * 8);
    dk[i] = *(const u32x4*)(dskip + dtile * 64 + oct * 8);
  }
#pragma unroll
  for (int i = 0; i < 2; ++i) tile_put(lds, (tid >> 3) + 32 * i, tid & 7, yv[i]);
  __syncthreads();
#pragma unroll
  for (int i = 0; i < 2; ++i) {
    const int oct = (lane >> 4) + 4 * i;
    const u32x4 y = tile_get_T(lds, lane, oct, wv);
    u32x4 o;
#pragma unroll
    for (int j = 0; j < 4; ++j) {
      const float y0 = __uint_as_float(y[j] << 16), y1 = __uint_as_float(y[j] & 0xffff0000u);
      const float u0 = __uint_as_float(uv[i][j] << 16), u1 = __uint_as_float(uv[i][j] & 0xffff0000u);
      const float d0 = __uint_as_float(dk[i][j] << 16), d1 = __uint_as_float(dk[i][j] & 0xffff0000u);
      o[j] = pack2(y0 + rbf(u0 * d0), y1 + rbf(u1 * d1));
    }
    *(u32x4*)(out + (size_t)row * OUTW + dtile * 64 + oct * 8) = o;
  }
}

constexpr int LDS1 = 34816;
constexpr int LDS2 = 44608;
constexpr int LDS3 = 448 * 128;

__global__ NOPK __launch_bounds__(256, 3) void k_l1(const u16* u, const u16* logAre, const u16* Aim, const u16* Cre, const u16* Cim,
                                               const u16* logdt, const u16* x, const u16* wqkv, const u16* wphi, char* ws) {
  __shared__ __attribute__((aligned(16))) char lds[LDS1];
  const int b = blockIdx.x;
  if (b < 256) { __builtin_amdgcn_s_setprio(3); role_b1(b, x, wqkv, wphi, ws, lds); }
  else if (b < 256 + 448) role_kgen(b - 256, logAre, Aim, Cre, Cim, logdt, ws, lds);
  else role_utrans(b - 704, u, ws, lds);
}
__global__ NOPK __launch_bounds__(256, 3) void k_l2(const u16* x, const u16* wqkv, const u16* wphi, char* ws) {
  __shared__ __attribute__((aligned(16))) char lds[LDS2];
  const int b = blockIdx.x;
  if (b < 448) { __builtin_amdgcn_s_setprio(3); role_conv(b, ws, lds); }
  else role_bmid(b - 448, wphi, ws, lds);
}
__global__ NOPK __launch_bounds__(256, 2) void k_l3(const u16* u, const u16* dskip, const u16* wo, u16* out, char* ws) {
  __shared__ __attribute__((aligned(16))) char lds[LDS3];
  const int b = blockIdx.x;
  if (b < 224) role_bout(b, wo, out, ws, lds);
  else role_aout(b - 224, u, dskip, out, ws, lds);
}

extern "C" void kernel_launch(void* const* d_in, const int* in_sizes, int n_in, void* d_out, int out_size, void* d_ws,
                              size_t ws_size, hipStream_t stream) {
  const u16* u = (const u16*)d_in[0];
  const u16* logAre = (const u16*)d_in[1];
  const u16* Aim = (const u16*)d_in[2];
  const u16* Cre = (const u16*)d_in[3];
  const u16* Cim = (const u16*)d_in[4];
  const u16* logdt = (const u16*)d_in[5];
  const u16* dskip = (const u16*)d_in[6];
  const u16* x = (const u16*)d_in[7];
  const u16* wqkv = (const u16*)d_in[8];
  const u16* wphi = (const u16*)d_in[9];
  const u16* wo = (const u16*)d_in[10];
  u16* out = (u16*)d_out;
  char* ws = (char*)d_ws;
  k_l1<<<448 + 256 + 224, 256, 0, stream>>>(u, logAre, Aim, Cre, Cim, logdt, x, wqkv, wphi, ws);
  k_l2<<<448 + 256, 256, 0, stream>>>(x, wqkv, wphi, ws);
  k_l3<<<224 + 224, 256, 0, stream>>>(u, dskip, wo, out, ws);
}
```
